```python
import torch

def _softmax(x, axis=-1):
    return torch.softmax(x, dim=axis)
def _gelu(x):
    return torch.nn.functional.gelu(x, approximate="tanh")
def _silu(x):
    return torch.nn.functional.silu(x)
def _sigmoid(x):
    return torch.sigmoid(x)
def _layernorm(x, axis=-1, eps=1e-5):
    m = x.mean(dim=axis, keepdim=True)
    v = x.var(dim=axis, keepdim=True, unbiased=False)
    return (x - m) / torch.sqrt(v + eps)
def _scatter_add(tgt, idx, src, axis=0):
    return tgt.clone().index_add_(axis, idx, src)
def _segment_sum(src, seg, n):
    out = torch.zeros((n,) + src.shape[1:], dtype=src.dtype)
    return out.index_add_(0, seg, src)
def _pad(x, pw):
    flat = tuple(v for pair in reversed(pw) for v in pair)
    return torch.nn.functional.pad(x, flat)

kernel_name = 'concat_heads__sliding_local_dilated__linear_kernel_hedgehog__prompt'

def setup_inputs():

    _w = (lambda a: (lambda t: t.to(torch.bfloat16) if t.is_floating_point() else t)(torch.as_tensor(a)))
    def _A_setup():
        B, S, D = 1, 2048, 640
        return {
            "x": _w((lambda *s: torch.randn(*s, dtype=torch.bfloat16) * 0.2)(B, S, D)),
            "wqkv": _w((lambda *s: torch.randn(*s, dtype=torch.bfloat16) * 0.2)(D, 3 * D)),
            "wo": _w((lambda *s: torch.randn(*s, dtype=torch.bfloat16) * 0.05)(D, D)),
        }
    _A_in = _A_setup()

    def _B_setup():
        B, S, D, H = 1, 2048, 640, 32
        Dh = D // H
        return {
            "x": _w((lambda *s: torch.randn(*s, dtype=torch.bfloat16) * 0.2)(B, S, D)),
            "wqkv": _w((lambda *s: torch.randn(*s, dtype=torch.bfloat16) * 0.8)(D, 3 * D)),
            "w_phi": _w((lambda *s: torch.randn(*s, dtype=torch.bfloat16) * 0.8)(H, Dh, Dh) * (Dh ** -0.5)),
            "wo": _w((lambda *s: torch.randn(*s, dtype=torch.bfloat16) * 0.2)(D, D)),
        }
    _B_in = _B_setup()

    return {
        "A_x": _A_in["x"],
        "A_wqkv": _A_in["wqkv"],
        "A_wo": _A_in["wo"],
        "B_x": _B_in["x"],
        "B_wqkv": _B_in["wqkv"],
        "B_w_phi": _B_in["w_phi"],
        "B_wo": _B_in["wo"],
    }

def reference(A_x, A_wqkv, A_wo, B_x, B_wqkv, B_w_phi, B_wo):
    def _A_ref(x, wqkv, wo):
        B, S, D = x.shape
        H = 32; Dh = D // H
        W, dil = 8, 2
        qkv = (lambda x, *d: x.permute(*d))((x @ wqkv).reshape(B, S, 3, H, Dh), 2, 0, 3, 1, 4)
        q, k, v = qkv[0], qkv[1], qkv[2]
        scale = Dh ** -0.5
        pos = torch.arange(S)
        off = torch.arange(W) * dil
        idx = pos[:, None] - off[None, :]
        mask = idx >= 0
        idx_c = torch.where(mask, idx, 0).reshape(-1)
        kg = k[:, :, idx_c, :].reshape(B, H, S, W, Dh)
        vg = v[:, :, idx_c, :].reshape(B, H, S, W, Dh)
        logits = torch.einsum("bhsd,bhswd->bhsw", q, kg) * scale
        logits = torch.where(mask[None, None, :, :], logits, -1e9)
        attn = _softmax(logits, axis=-1)
        o = torch.einsum("bhsw,bhswd->bhsd", attn, vg)
        return (lambda x, *d: x.permute(*d))(o, 0, 2, 1, 3).reshape(B, S, D) @ wo

    def _B_ref(x, wqkv, w_phi, wo):
        B, S, D = x.shape
        H = 32; Dh = D // H
        qkv = (lambda x, *d: x.permute(*d))((x @ wqkv).reshape(B, S, 3, H, Dh), 2, 0, 3, 1, 4)
        q, k, v = qkv[0], qkv[1], qkv[2]
        pq = torch.einsum("bhsd,hdf->bhsf", q, w_phi)
        pk = torch.einsum("bhsd,hdf->bhsf", k, w_phi)
        phi_q = torch.concatenate([_softmax(pq, axis=-1), _softmax(-pq, axis=-1)], axis=-1)
        phi_k = torch.concatenate([_softmax(pk, axis=-1), _softmax(-pk, axis=-1)], axis=-1)
        kv = torch.einsum("bhsf,bhsd->bhfd", phi_k, v)
        z  = torch.einsum("bhsf->bhf", phi_k)
        num = torch.einsum("bhqf,bhfd->bhqd", phi_q, kv)
        den = torch.einsum("bhqf,bhf->bhq", phi_q, z)[..., None] + 1e-6
        o = num / den
        return (lambda x, *d: x.permute(*d))(o, 0, 2, 1, 3).reshape(B, S, D) @ wo

    a = _A_ref(A_x, A_wqkv, A_wo)
    b = _B_ref(B_x, B_wqkv, B_w_phi, B_wo)
    return torch.concatenate([a, b], axis=-1)
```

```cpp
#include <hip/hip_runtime.h>
#include <hip/hip_bf16.h>
typedef __hip_bfloat16 bf;
__device__ __forceinline__ float b2f(bf v){return __bfloat162float(v);}
__device__ __forceinline__ float rb(float v){return __bfloat162float(__float2bfloat16(v));}
__global__ void gemm(const bf* A, const bf* B, bf* C, int M, int N, int K, int ldc){
  __shared__ float As[16][17], Bs[16][17];
  int tx=threadIdx.x&15, ty=threadIdx.x>>4;
  int r=blockIdx.y*16+ty, c=blockIdx.x*16+tx;
  float acc=0;
  for(int k0=0;k0<K;k0+=16){
    As[ty][tx]=b2f(A[r*K+k0+tx]); Bs[ty][tx]=b2f(B[(k0+ty)*N+c]);
    __syncthreads();
    for(int k=0;k<16;k++) acc+=As[ty][k]*Bs[k][tx];
    __syncthreads();
  }
  C[r*ldc+c]=__float2bfloat16(acc);
}
__global__ void attnA(const bf* qkv, bf* o){
  int i=blockIdx.x*blockDim.x+threadIdx.x; int s=i/32,h=i%32;
  const bf* q=qkv+s*1920+h*20;
  float lg[8]; float m=-1e30f;
  #pragma unroll
  for(int w=0;w<8;w++){int t=s-2*w; float a=0; int tc=t>=0?t:0; const bf* k=qkv+tc*1920+640+h*20;
    for(int d=0;d<20;d++) a+=b2f(q[d])*b2f(k[d]);
    a=rb(rb(a)*rb(0.22360679775f)); if(t<0)a=-1e9f; lg[w]=a; m=fmaxf(m,a);}
  float sum=0;
  #pragma unroll
  for(int w=0;w<8;w++){lg[w]=__expf(lg[w]-m); sum+=lg[w];}
  #pragma unroll
  for(int w=0;w<8;w++) lg[w]=rb(lg[w]/sum);
  for(int d=0;d<20;d++){float a=0;
    #pragma unroll
    for(int w=0;w<8;w++){int t=s-2*w; int tc=t>=0?t:0; a+=lg[w]*b2f(qkv[tc*1920+1280+h*20+d]);}
    o[s*640+h*20+d]=__float2bfloat16(a);}
}
__global__ void phiB(const bf* qkv, const bf* wphi, float* phi){
  int i=blockIdx.x*blockDim.x+threadIdx.x; int which=i/(2048*32); int r=i%(2048*32); int s=r/32,h=r%32;
  const bf* q=qkv+s*1920+which*640+h*20;
  float* out=phi+(size_t)i*40;
  float mx=-1e30f,mn=1e30f;
  for(int f=0;f<20;f++){float a=0; for(int d=0;d<20;d++) a+=b2f(q[d])*b2f(wphi[h*400+d*20+f]); a=rb(a); out[f]=a; mx=fmaxf(mx,a); mn=fminf(mn,a);}
  float s1=0,s2=0;
  for(int f=0;f<20;f++){float a=out[f]; float e1=__expf(a-mx), e2=__expf(mn-a); out[f]=e1; out[20+f]=e2; s1+=e1; s2+=e2;}
  for(int f=0;f<20;f++){out[f]=rb(out[f]/s1); out[20+f]=rb(out[20+f]/s2);}
}
__global__ void kvB(const bf* qkv, const float* phi, float* kv){
  int i=blockIdx.x*blockDim.x+threadIdx.x; if(i>=32*40*21) return; int h=i/840, f=(i%840)/21, d=i%21;
  const float* pk=phi+(size_t)2048*32*40;
  float a=0;
  for(int s=0;s<2048;s++){ float v = d<20? b2f(qkv[s*1920+1280+h*20+d]) : 1.f; a+=pk[(s*32+h)*40+f]*v;}
  kv[i]=rb(a);
}
__global__ void oB(const float* phi, const float* kv, bf* o){
  int i=blockIdx.x*blockDim.x+threadIdx.x; int s=i/32,h=i%32;
  const float* pq=phi+(size_t)i*40; const float* K=kv+h*840;
  float den=0; for(int f=0;f<40;f++) den+=pq[f]*K[f*21+20]; den=rb(rb(den)+1e-6f);
  for(int d=0;d<20;d++){float a=0; for(int f=0;f<40;f++) a+=pq[f]*K[f*21+d]; o[s*640+h*20+d]=__float2bfloat16(rb(a)/den);}
}
extern "C" void kernel_launch(void* const* d_in, const int* in_sizes, int n_in, void* d_out, int out_size, void* d_ws, size_t ws_size, hipStream_t stream){
  const bf* Ax=(const bf*)d_in[0]; const bf* Aw=(const bf*)d_in[1]; const bf* Ao=(const bf*)d_in[2];
  const bf* Bx=(const bf*)d_in[3]; const bf* Bw=(const bf*)d_in[4]; const bf* Bp=(const bf*)d_in[5]; const bf* Bo=(const bf*)d_in[6];
  bf* out=(bf*)d_out; char* ws=(char*)d_ws;
  bf* qkvA=(bf*)ws; bf* qkvB=(bf*)(ws+(8<<20)); bf* oA=(bf*)(ws+(16<<20)); bf* oBb=(bf*)(ws+(20<<20)); float* phi=(float*)(ws+(24<<20)); float* kv=(float*)(ws+(48<<20));
  gemm<<<dim3(120,128),256,0,stream>>>(Ax,Aw,qkvA,2048,1920,640,1920);
  gemm<<<dim3(120,128),256,0,stream>>>(Bx,Bw,qkvB,2048,1920,640,1920);
  attnA<<<256,256,0,stream>>>(qkvA,oA);
  phiB<<<512,256,0,stream>>>(qkvB,Bp,phi);
  kvB<<<105,256,0,stream>>>(qkvB,phi,kv);
  oB<<<256,256,0,stream>>>(phi,kv,oBb);
  gemm<<<dim3(40,128),256,0,stream>>>(oA,Ao,out,2048,640,640,1280);
  gemm<<<dim3(40,128),256,0,stream>>>(oBb,Bo,out+640,2048,640,640,1280);
}
```

```cpp
#include <hip/hip_runtime.h>
#include <hip/hip_bf16.h>

typedef unsigned long long u64;
typedef unsigned u32;
typedef float f32x2 __attribute__((ext_vector_type(2)));
typedef float f32x4 __attribute__((ext_vector_type(4)));
typedef float f32x8 __attribute__((ext_vector_type(8)));
typedef float f32x16 __attribute__((ext_vector_type(16)));
typedef float f32x32 __attribute__((ext_vector_type(32)));
typedef __bf16 bf16x2 __attribute__((ext_vector_type(2)));
typedef short s16x4 __attribute__((ext_vector_type(4)));
typedef u32 u32x2 __attribute__((ext_vector_type(2)));
typedef u32 u32x4 __attribute__((ext_vector_type(4)));
typedef u64 u64x2 __attribute__((ext_vector_type(2)));
typedef u64 u64x4 __attribute__((ext_vector_type(4)));
typedef u64 u64x8 __attribute__((ext_vector_type(8)));
#define LAS __attribute__((address_space(3)))
#define GAS __attribute__((address_space(1)))
#define DI __device__ __forceinline__
#define RLX_AGENT __ATOMIC_RELAXED, __HIP_MEMORY_SCOPE_AGENT

constexpr u32 L_CNT = 81920, L_WO = L_CNT + 64  , L_HS = L_CNT + 128, L_FREE = L_CNT + 224, L_STG = L_FREE + 16, L_DUM = L_CNT + 256, L_ZERO = L_CNT + 544, L_WPHI = 83968;
constexpr u32 L_X = 87552, O_K = 5376, O_V = 10752, L_SP = 153088, L_KV = L_SP, L_Z2 = L_SP + 7168;
constexpr u32 RS = 168;
constexpr size_t W_EPOCH = 0, W_FK = 4096, W_FO = 131072, W_HALO = 524288, W_KVR = 917504 + 65536, W_KVP = 1u << 20, W_O = 2u << 20, W_F2 = 15u << 19, W_DBG = 16u << 20;
constexpr int CODE_BYTES = 38300;
#define PSL 2
#define PD1 6
#define PD2 11
constexpr int RETRIES = 1 << 14;
constexpr u32 SPIN_CAP = 1u << 16;

DI u32 rfl(u32 v) { return __builtin_amdgcn_readfirstlane(v); }
DI u32 cvtpk(float lo, float hi) { f32x2 v = {lo, hi}; return __builtin_bit_cast(u32, __builtin_convertvector(v, bf16x2)); }
template <class T> DI T ldsr(u32 a) { return *(LAS T*)(size_t)a; }
template <class T> DI void ldsw(u32 a, T v) { *(LAS T*)(size_t)a = v; }
DI u32x2 trrd(u32 a) { return __builtin_bit_cast(u32x2, __builtin_amdgcn_ds_read_tr16_b64_v4i16((LAS s16x4*)(size_t)a)); }
DI u32x4 cat(u32x2 a, u32x2 b) { return __builtin_shufflevector(a, b, 0, 1, 2, 3); }
DI float max3f(float a, float b, float c) { float r; asm("v_max3_f32 %0, %1, %2, %3" : "=v"(r) : "v"(a), "v"(b), "v"(c)); return r; }
DI float min3f(float a, float b, float c) { float r; asm("v_min3_f32 %0, %1, %2, %3" : "=v"(r) : "v"(a), "v"(b), "v"(c)); return r; }
DI float max2n(float a, float b) { float r; asm("v_max_f32_e32 %0, %1, %2\n\ts_nop 1" : "=v"(r) : "v"(a), "v"(b)); return r; }
DI float max2f(float a, float b) { float r; asm("v_max_f32_e32 %0, %1, %2" : "=v"(r) : "v"(a), "v"(b)); return r; }

#define MID(i)
#define TSx(i)
#define TS_OUT()
#define MIDIN
#define DBG_OUT()
#define MID_OUT()
#define MCLOB
#define STAMP(i) do { } while (0)

#define FSx(i)
#define KV_BODY \
  "s_mov_b32 s75, 0x100000\n" \
  "LK0_s:\n" \
  "ds_read_b32 v11, v8 offset:0\n" \
  "s_sub_i32 s75, s75, 1\n" \
  "s_cmp_lt_i32 s75, 0\n" \
  "s_cbranch_scc1 LK0_k\n" \
  "s_waitcnt lgkmcnt(0)\n" \
  "v_cmp_gt_u32_e32 vcc, 1, v11\n" \
  "s_cbranch_vccnz LK0_s\n" \
  "LK0_k:\n" \
  "ds_read_b128 a[0:3], v6 offset:0\n" \
  "ds_read_b128 a[4:7], v6 offset:2048\n" \
  "ds_read_b128 a[8:11], v7 offset:0\n" \
  "ds_read_b128 a[12:15], v7 offset:2048\n" \
  "ds_read_b64_tr_b16 v[12:13], v4 offset:0\n" \
  "ds_read_b64_tr_b16 v[14:15], v4 offset:1280\n" \
  "ds_read_b64_tr_b16 v[16:17], v4 offset:8\n" \
  "ds_read_b64_tr_b16 v[18:19], v4 offset:1288\n" \
  "ds_read_b64_tr_b16 v[20:21], v4 offset:16\n" \
  "ds_read_b64_tr_b16 v[22:23], v4 offset:1296\n" \
  "ds_read_b64_tr_b16 v[24:25], v4 offset:24\n" \
  "ds_read_b64_tr_b16 v[26:27], v4 offset:1304\n" \
  "s_waitcnt lgkmcnt(6)\n" \
  "v_mfma_f32_16x16x32_bf16 v[48:51], v[12:15], a[0:3], 0\n" \
  "ds_read_b64_tr_b16 v[28:29], v4 offset:32\n" \
  "v_mfma_f32_16x16x32_bf16 v[68:71], v[12:15], a[4:7], 0\n" \
  "ds_read_b64_tr_b16 v[30:31], v4 offset:1312\n" \
  "s_waitcnt lgkmcnt(6)\n" \
  "v_mfma_f32_16x16x32_bf16 v[52:55], v[16:19], a[0:3], 0\n" \
  "ds_read_b64_tr_b16 v[12:13], v4 offset:10240\n" \
  TSx(24) \
  "v_mfma_f32_16x16x32_bf16 v[72:75], v[16:19], a[4:7], 0\n" \
  "ds_read_b64_tr_b16 v[14:15], v4 offset:11520\n" \
  "s_waitcnt lgkmcnt(6)\n" \
  "v_mfma_f32_16x16x32_bf16 v[56:59], v[20:23], a[0:3], 0\n" \
  "ds_read_b64_tr_b16 v[16:17], v4 offset:10248\n" \
  "v_mfma_f32_16x16x32_bf16 v[76:79], v[20:23], a[4:7], 0\n" \
  "ds_read_b64_tr_b16 v[18:19], v4 offset:11528\n" \
  "s_waitcnt lgkmcnt(6)\n" \
  "v_mfma_f32_16x16x32_bf16 v[60:63], v[24:27], a[0:3], 0\n" \
  "ds_read_b64_tr_b16 v[20:21], v4 offset:10256\n" \
  "v_mfma_f32_16x16x32_bf16 v[80:83], v[24:27], a[4:7], 0\n" \
  "ds_read_b64_tr_b16 v[22:23], v4 offset:11536\n" \
  "s_waitcnt lgkmcnt(6)\n" \
  "v_mfma_f32_16x16x32_bf16 v[64:67], v[28:31], a[0:3], 0\n" \
  "ds_read_b64_tr_b16 v[24:25], v4 offset:10264\n" \
  "v_mfma_f32_16x16x32_bf16 v[84:87], v[28:31], a[4:7], 0\n" \
  "ds_read_b64_tr_b16 v[26:27], v4 offset:11544\n" \
  "s_waitcnt lgkmcnt(6)\n" \
  "v_mfma_f32_16x16x32_bf16 v[88:91], v[12:15], a[0:3], 0\n" \
  "ds_read_b64_tr_b16 v[28:29], v4 offset:10272\n" \
  "v_mfma_f32_16x16x32_bf16 v[108:111], v[12:15], a[4:7], 0\n" \
  "ds_read_b64_tr_b16 v[30:31], v4 offset:11552\n" \
  "s_waitcnt lgkmcnt(6)\n" \
  "v_mfma_f32_16x16x32_bf16 v[92:95], v[16:19], a[0:3], 0\n" \
  "ds_read_b64_tr_b16 v[12:13], v4 offset:5120\n" \
  "v_mfma_f32_16x16x32_bf16 v[112:115], v[16:19], a[4:7], 0\n" \
  "ds_read_b64_tr_b16 v[14:15], v4 offset:6400\n" \
  "s_waitcnt lgkmcnt(6)\n" \
  "v_mfma_f32_16x16x32_bf16 v[96:99], v[20:23], a[0:3], 0\n" \
  "ds_read_b64_tr_b16 v[16:17], v4 offset:5128\n" \
  "v_mfma_f32_16x16x32_bf16 v[116:119], v[20:23], a[4:7], 0\n" \
  "ds_read_b64_tr_b16 v[18:19], v4 offset:6408\n" \
  "s_waitcnt lgkmcnt(6)\n" \
  "v_mfma_f32_16x16x32_bf16 v[100:103], v[24:27], a[0:3], 0\n" \
  "ds_read_b64_tr_b16 v[20:21], v4 offset:5136\n" \
  "ds_read_b32 v11, v8 offset:4\n" \
  "v_mfma_f32_16x16x32_bf16 v[120:123], v[24:27], a[4:7], 0\n" \
  "ds_read_b64_tr_b16 v[22:23], v4 offset:6416\n" \
  "s_waitcnt lgkmcnt(7)\n" \
  "v_mfma_f32_16x16x32_bf16 v[104:107], v[28:31], a[0:3], 0\n" \
  "ds_read_b64_tr_b16 v[24:25], v4 offset:5144\n" \
  "v_mfma_f32_16x16x32_bf16 v[124:127], v[28:31], a[4:7], 0\n" \
  "ds_read_b64_tr_b16 v[26:27], v4 offset:6424\n" \
  "s_waitcnt lgkmcnt(7)\n" \
  "v_mfma_f32_16x16x32_bf16 v[48:51], v[12:15], a[8:11], v[48:51]\n" \
  "ds_read_b64_tr_b16 v[28:29], v4 offset:5152\n" \
  "v_mfma_f32_16x16x32_bf16 v[68:71], v[12:15], a[12:15], v[68:71]\n" \
  "ds_read_b64_tr_b16 v[30:31], v4 offset:6432\n" \
  "s_waitcnt lgkmcnt(7)\n" \
  "v_mfma_f32_16x16x32_bf16 v[52:55], v[16:19], a[8:11], v[52:55]\n" \
  "ds_read_b64_tr_b16 v[12:13], v4 offset:15360\n" \
  "v_mfma_f32_16x16x32_bf16 v[72:75], v[16:19], a[12:15], v[72:75]\n" \
  "ds_read_b64_tr_b16 v[14:15], v4 offset:16640\n" \
  "s_waitcnt lgkmcnt(6)\n" \
  "v_mfma_f32_16x16x32_bf16 v[56:59], v[20:23], a[8:11], v[56:59]\n" \
  "ds_read_b64_tr_b16 v[16:17], v4 offset:15368\n" \
  "v_mfma_f32_16x16x32_bf16 v[76:79], v[20:23], a[12:15], v[76:79]\n" \
  "ds_read_b64_tr_b16 v[18:19], v4 offset:16648\n" \
  "s_waitcnt lgkmcnt(6)\n" \
  "v_mfma_f32_16x16x32_bf16 v[60:63], v[24:27], a[8:11], v[60:63]\n" \
  "ds_read_b64_tr_b16 v[20:21], v4 offset:15376\n" \
  "v_cmp_gt_u32_e32 vcc, 1, v11\n" \
  "v_mfma_f32_16x16x32_bf16 v[80:83], v[24:27], a[12:15], v[80:83]\n" \
  "ds_read_b64_tr_b16 v[22:23], v4 offset:16656\n" \
  "s_waitcnt lgkmcnt(6)\n" \
  "v_mfma_f32_16x16x32_bf16 v[64:67], v[28:31], a[8:11], v[64:67]\n" \
  "ds_read_b64_tr_b16 v[24:25], v4 offset:15384\n" \
  "s_cbranch_vccnz LK1_s\n" \
  "LK1_k:\n" \
  "v_mfma_f32_16x16x32_bf16 v[84:87], v[28:31], a[12:15], v[84:87]\n" \
  "ds_read_b64_tr_b16 v[26:27], v4 offset:16664\n" \
  "s_waitcnt lgkmcnt(6)\n" \
  "v_mfma_f32_16x16x32_bf16 v[88:91], v[12:15], a[8:11], v[88:91]\n" \
  "ds_read_b64_tr_b16 v[28:29], v4 offset:15392\n" \
  "ds_read_b128 a[16:19], v6 offset:4096\n" \
  "v_mfma_f32_16x16x32_bf16 v[108:111], v[12:15], a[12:15], v[108:111]\n" \
  "ds_read_b64_tr_b16 v[30:31], v4 offset:16672\n" \
  "s_waitcnt lgkmcnt(7)\n" \
  "v_mfma_f32_16x16x32_bf16 v[92:95], v[16:19], a[8:11], v[92:95]\n" \
  "ds_read_b64_tr_b16 v[12:13], v4 offset:20480\n" \
  "ds_read_b128 a[20:23], v6 offset:6144\n" \
  "v_mfma_f32_16x16x32_bf16 v[112:115], v[16:19], a[12:15], v[112:115]\n" \
  "ds_read_b64_tr_b16 v[14:15], v4 offset:21760\n" \
  "s_waitcnt lgkmcnt(8)\n" \
  "v_mfma_f32_16x16x32_bf16 v[96:99], v[20:23], a[8:11], v[96:99]\n" \
  "ds_read_b64_tr_b16 v[16:17], v4 offset:20488\n" \
  "ds_read_b128 a[24:27], v7 offset:4096\n" \
  "v_mfma_f32_16x16x32_bf16 v[116:119], v[20:23], a[12:15], v[116:119]\n" \
  "ds_read_b64_tr_b16 v[18:19], v4 offset:21768\n" \
  "s_waitcnt lgkmcnt(9)\n" \
  "v_mfma_f32_16x16x32_bf16 v[100:103], v[24:27], a[8:11], v[100:103]\n" \
  "ds_read_b64_tr_b16 v[20:21], v4 offset:20496\n" \
  "ds_read_b128 a[28:31], v7 offset:6144\n" \
  "v_mfma_f32_16x16x32_bf16 v[120:123], v[24:27], a[12:15], v[120:123]\n" \
  "ds_read_b64_tr_b16 v[22:23], v4 offset:21776\n" \
  "s_waitcnt lgkmcnt(9)\n" \
  "v_mfma_f32_16x16x32_bf16 v[104:107], v[28:31], a[8:11], v[104:107]\n" \
  "ds_read_b64_tr_b16 v[24:25], v4 offset:20504\n" \
  "v_mfma_f32_16x16x32_bf16 v[124:127], v[28:31], a[12:15], v[124:127]\n" \
  "ds_read_b64_tr_b16 v[26:27], v4 offset:21784\n" \
  "s_waitcnt lgkmcnt(8)\n" \
  "v_mfma_f32_16x16x32_bf16 v[48:51], v[12:15], a[16:19], v[48:51]\n" \
  "ds_read_b64_tr_b16 v[28:29], v4 offset:20512\n" \
  "ds_add_u32 v3, v2\n" \
  "v_mfma_f32_16x16x32_bf16 v[68:71], v[12:15], a[20:23], v[68:71]\n" \
  "ds_read_b64_tr_b16 v[30:31], v4 offset:21792\n" \
  "s_waitcnt lgkmcnt(8)\n" \
  "v_mfma_f32_16x16x32_bf16 v[52:55], v[16:19], a[16:19], v[52:55]\n" \
  "ds_read_b64_tr_b16 v[12:13], v4 offset:30720\n" \
  TSx(26) \
  "v_mfma_f32_16x16x32_bf16 v[72:75], v[16:19], a[20:23], v[72:75]\n" \
  "ds_read_b64_tr_b16 v[14:15], v4 offset:32000\n" \
  "s_waitcnt lgkmcnt(7)\n" \
  "v_mfma_f32_16x16x32_bf16 v[56:59], v[20:23], a[16:19], v[56:59]\n" \
  "ds_read_b64_tr_b16 v[16:17], v4 offset:30728\n" \
  "v_mfma_f32_16x16x32_bf16 v[76:79], v[20:23], a[20:23], v[76:79]\n" \
  "ds_read_b64_tr_b16 v[18:19], v4 offset:32008\n" \
  "s_waitcnt lgkmcnt(7)\n" \
  "v_mfma_f32_16x16x32_bf16 v[60:63], v[24:27], a[16:19], v[60:63]\n" \
  "ds_read_b64_tr_b16 v[20:21], v4 offset:30736\n" \
  "v_mfma_f32_16x16x32_bf16 v[80:83], v[24:27], a[20:23], v[80:83]\n" \
  "ds_read_b64_tr_b16 v[22:23], v4 offset:32016\n" \
  "s_waitcnt lgkmcnt(6)\n" \
  "v_mfma_f32_16x16x32_bf16 v[64:67], v[28:31], a[16:19], v[64:67]\n" \
  "ds_read_b64_tr_b16 v[24:25], v4 offset:30744\n" \
  "v_mfma_f32_16x16x32_bf16 v[84:87], v[28:31], a[20:23], v[84:87]\n" \
  "ds_read_b64_tr_b16 v[26:27], v4 offset:32024\n" \
  "s_waitcnt lgkmcnt(6)\n" \
  "v_mfma_f32_16x16x32_bf16 v[88:91], v[12:15], a[16:19], v[88:91]\n" \
  "ds_read_b64_tr_b16 v[28:29], v4 offset:30752\n" \
  "v_mfma_f32_16x16x32_bf16 v[108:111], v[12:15], a[20:23], v[108:111]\n" \
  "ds_read_b64_tr_b16 v[30:31], v4 offset:32032\n" \
  "s_waitcnt lgkmcnt(6)\n" \
  "v_mfma_f32_16x16x32_bf16 v[92:95], v[16:19], a[16:19], v[92:95]\n" \
  "ds_read_b64_tr_b16 v[12:13], v4 offset:25600\n" \
  "v_mfma_f32_16x16x32_bf16 v[112:115], v[16:19], a[20:23], v[112:115]\n" \
  "ds_read_b64_tr_b16 v[14:15], v4 offset:26880\n" \
  "s_waitcnt lgkmcnt(6)\n" \
  "v_mfma_f32_16x16x32_bf16 v[96:99], v[20:23], a[16:19], v[96:99]\n" \
  "ds_read_b64_tr_b16 v[16:17], v4 offset:25608\n" \
  "v_mfma_f32_16x16x32_bf16 v[116:119], v[20:23], a[20:23], v[116:119]\n" \
  "ds_read_b64_tr_b16 v[18:19], v4 offset:26888\n" \
  "s_waitcnt lgkmcnt(6)\n" \
  "v_mfma_f32_16x16x32_bf16 v[100:103], v[24:27], a[16:19], v[100:103]\n" \
  "ds_read_b64_tr_b16 v[20:21], v4 offset:25616\n" \
  "ds_read_b32 v11, v8 offset:8\n" \
  "v_mfma_f32_16x16x32_bf16 v[120:123], v[24:27], a[20:23], v[120:123]\n" \
  "ds_read_b64_tr_b16 v[22:23], v4 offset:26896\n" \
  "s_waitcnt lgkmcnt(7)\n" \
  "v_mfma_f32_16x16x32_bf16 v[104:107], v[28:31], a[16:19], v[104:107]\n" \
  "ds_read_b64_tr_b16 v[24:25], v4 offset:25624\n" \
  "v_mfma_f32_16x16x32_bf16 v[124:127], v[28:31], a[20:23], v[124:127]\n" \
  "ds_read_b64_tr_b16 v[26:27], v4 offset:26904\n" \
  "s_waitcnt lgkmcnt(7)\n" \
  "v_mfma_f32_16x16x32_bf16 v[48:51], v[12:15], a[24:27], v[48:51]\n" \
  "ds_read_b64_tr_b16 v[28:29], v4 offset:25632\n" \
  "v_mfma_f32_16x16x32_bf16 v[68:71], v[12:15], a[28:31], v[68:71]\n" \
  "ds_read_b64_tr_b16 v[30:31], v4 offset:26912\n" \
  "s_waitcnt lgkmcnt(7)\n" \
  "v_mfma_f32_16x16x32_bf16 v[52:55], v[16:19], a[24:27], v[52:55]\n" \
  "ds_read_b64_tr_b16 v[12:13], v4 offset:35840\n" \
  "v_mfma_f32_16x16x32_bf16 v[72:75], v[16:19], a[28:31], v[72:75]\n" \
  "ds_read_b64_tr_b16 v[14:15], v4 offset:37120\n" \
  "s_waitcnt lgkmcnt(6)\n" \
  "v_mfma_f32_16x16x32_bf16 v[56:59], v[20:23], a[24:27], v[56:59]\n" \
  "ds_read_b64_tr_b16 v[16:17], v4 offset:35848\n" \
  "v_mfma_f32_16x16x32_bf16 v[76:79], v[20:23], a[28:31], v[76:79]\n" \
  "ds_read_b64_tr_b16 v[18:19], v4 offset:37128\n" \
  "s_waitcnt lgkmcnt(6)\n" \
  "v_mfma_f32_16x16x32_bf16 v[60:63], v[24:27], a[24:27], v[60:63]\n" \
  "ds_read_b64_tr_b16 v[20:21], v4 offset:35856\n" \
  "v_cmp_gt_u32_e32 vcc, 1, v11\n" \
  "v_mfma_f32_16x16x32_bf16 v[80:83], v[24:27], a[28:31], v[80:83]\n" \
  "ds_read_b64_tr_b16 v[22:23], v4 offset:37136\n" \
  "s_waitcnt lgkmcnt(6)\n" \
  "v_mfma_f32_16x16x32_bf16 v[64:67], v[28:31], a[24:27], v[64:67]\n" \
  "ds_read_b64_tr_b16 v[24:25], v4 offset:35864\n" \
  "s_cbranch_vccnz LK2_s\n" \
  "LK2_k:\n" \
  "v_mfma_f32_16x16x32_bf16 v[84:87], v[28:31], a[28:31], v[84:87]\n" \
  "ds_read_b64_tr_b16 v[26:27], v4 offset:37144\n" \
  "s_waitcnt lgkmcnt(6)\n" \
  "v_mfma_f32_16x16x32_bf16 v[88:91], v[12:15], a[24:27], v[88:91]\n" \
  "ds_read_b64_tr_b16 v[28:29], v4 offset:35872\n" \
  "ds_read_b128 a[32:35], v6 offset:8192\n" \
  "v_mfma_f32_16x16x32_bf16 v[108:111], v[12:15], a[28:31], v[108:111]\n" \
  "ds_read_b64_tr_b16 v[30:31], v4 offset:37152\n" \
  "s_waitcnt lgkmcnt(7)\n" \
  "v_mfma_f32_16x16x32_bf16 v[92:95], v[16:19], a[24:27], v[92:95]\n" \
  "ds_read_b64_tr_b16 v[12:13], v4 offset:40960\n" \
  "ds_read_b128 a[36:39], v6 offset:10240\n" \
  "v_mfma_f32_16x16x32_bf16 v[112:115], v[16:19], a[28:31], v[112:115]\n" \
  "ds_read_b64_tr_b16 v[14:15], v4 offset:42240\n" \
  "s_waitcnt lgkmcnt(8)\n" \
  "v_mfma_f32_16x16x32_bf16 v[96:99], v[20:23], a[24:27], v[96:99]\n" \
  "ds_read_b64_tr_b16 v[16:17], v4 offset:40968\n" \
  "ds_read_b128 a[40:43], v7 offset:8192\n" \
  "v_mfma_f32_16x16x32_bf16 v[116:119], v[20:23], a[28:31], v[116:119]\n" \
  "ds_read_b64_tr_b16 v[18:19], v4 offset:42248\n" \
  "s_waitcnt lgkmcnt(9)\n" \
  "v_mfma_f32_16x16x32_bf16 v[100:103], v[24:27], a[24:27], v[100:103]\n" \
  "ds_read_b64_tr_b16 v[20:21], v4 offset:40976\n" \
  "ds_read_b128 a[44:47], v7 offset:10240\n" \
  "v_mfma_f32_16x16x32_bf16 v[120:123], v[24:27], a[28:31], v[120:123]\n" \
  "ds_read_b64_tr_b16 v[22:23], v4 offset:42256\n" \
  "s_waitcnt lgkmcnt(9)\n" \
  "v_mfma_f32_16x16x32_bf16 v[104:107], v[28:31], a[24:27], v[104:107]\n" \
  "ds_read_b64_tr_b16 v[24:25], v4 offset:40984\n" \
  "v_mfma_f32_16x16x32_bf16 v[124:127], v[28:31], a[28:31], v[124:127]\n" \
  "ds_read_b64_tr_b16 v[26:27], v4 offset:42264\n" \
  "s_waitcnt lgkmcnt(8)\n" \
  "v_mfma_f32_16x16x32_bf16 v[48:51], v[12:15], a[32:35], v[48:51]\n" \
  "ds_read_b64_tr_b16 v[28:29], v4 offset:40992\n" \
  "ds_add_u32 v3, v2\n" \
  "v_mfma_f32_16x16x32_bf16 v[68:71], v[12:15], a[36:39], v[68:71]\n" \
  "ds_read_b64_tr_b16 v[30:31], v4 offset:42272\n" \
  "s_waitcnt lgkmcnt(8)\n" \
  "v_mfma_f32_16x16x32_bf16 v[52:55], v[16:19], a[32:35], v[52:55]\n" \
  "ds_read_b64_tr_b16 v[12:13], v4 offset:51200\n" \
  TSx(28) \
  "v_mfma_f32_16x16x32_bf16 v[72:75], v[16:19], a[36:39], v[72:75]\n" \
  "ds_read_b64_tr_b16 v[14:15], v4 offset:52480\n" \
  "s_waitcnt lgkmcnt(7)\n" \
  "v_mfma_f32_16x16x32_bf16 v[56:59], v[20:23], a[32:35], v[56:59]\n" \
  "ds_read_b64_tr_b16 v[16:17], v4 offset:51208\n" \
  "v_mfma_f32_16x16x32_bf16 v[76:79], v[20:23], a[36:39], v[76:79]\n" \
  "ds_read_b64_tr_b16 v[18:19], v4 offset:52488\n" \
  "s_waitcnt lgkmcnt(7)\n" \
  "v_mfma_f32_16x16x32_bf16 v[60:63], v[24:27], a[32:35], v[60:63]\n" \
  "ds_read_b64_tr_b16 v[20:21], v4 offset:51216\n" \
  "v_mfma_f32_16x16x32_bf16 v[80:83], v[24:27], a[36:39], v[80:83]\n" \
  "ds_read_b64_tr_b16 v[22:23], v4 offset:52496\n" \
  "s_waitcnt lgkmcnt(6)\n" \
  "v_mfma_f32_16x16x32_bf16 v[64:67], v[28:31], a[32:35], v[64:67]\n" \
  "ds_read_b64_tr_b16 v[24:25], v4 offset:51224\n" \
  "v_mfma_f32_16x16x32_bf16 v[84:87], v[28:31], a[36:39], v[84:87]\n" \
  "ds_read_b64_tr_b16 v[26:27], v4 offset:52504\n" \
  "s_waitcnt lgkmcnt(6)\n" \
  "v_mfma_f32_16x16x32_bf16 v[88:91], v[12:15], a[32:35], v[88:91]\n" \
  "ds_read_b64_tr_b16 v[28:29], v4 offset:51232\n" \
  "v_mfma_f32_16x16x32_bf16 v[108:111], v[12:15], a[36:39], v[108:111]\n" \
  "ds_read_b64_tr_b16 v[30:31], v4 offset:52512\n" \
  "s_waitcnt lgkmcnt(6)\n" \
  "v_mfma_f32_16x16x32_bf16 v[92:95], v[16:19], a[32:35], v[92:95]\n" \
  "ds_read_b64_tr_b16 v[12:13], v4 offset:46080\n" \
  "v_mfma_f32_16x16x32_bf16 v[112:115], v[16:19], a[36:39], v[112:115]\n" \
  "ds_read_b64_tr_b16 v[14:15], v4 offset:47360\n" \
  "s_waitcnt lgkmcnt(6)\n" \
  "v_mfma_f32_16x16x32_bf16 v[96:99], v[20:23], a[32:35], v[96:99]\n" \
  "ds_read_b64_tr_b16 v[16:17], v4 offset:46088\n" \
  "v_mfma_f32_16x16x32_bf16 v[116:119], v[20:23], a[36:39], v[116:119]\n" \
  "ds_read_b64_tr_b16 v[18:19], v4 offset:47368\n" \
  "s_waitcnt lgkmcnt(6)\n" \
  "v_mfma_f32_16x16x32_bf16 v[100:103], v[24:27], a[32:35], v[100:103]\n" \
  "ds_read_b64_tr_b16 v[20:21], v4 offset:46096\n" \
  "ds_read_b32 v11, v8 offset:12\n" \
  "v_mfma_f32_16x16x32_bf16 v[120:123], v[24:27], a[36:39], v[120:123]\n" \
  "ds_read_b64_tr_b16 v[22:23], v4 offset:47376\n" \
  "s_waitcnt lgkmcnt(7)\n" \
  "v_mfma_f32_16x16x32_bf16 v[104:107], v[28:31], a[32:35], v[104:107]\n" \
  "ds_read_b64_tr_b16 v[24:25], v4 offset:46104\n" \
  "v_mfma_f32_16x16x32_bf16 v[124:127], v[28:31], a[36:39], v[124:127]\n" \
  "ds_read_b64_tr_b16 v[26:27], v4 offset:47384\n" \
  "s_waitcnt lgkmcnt(7)\n" \
  "v_mfma_f32_16x16x32_bf16 v[48:51], v[12:15], a[40:43], v[48:51]\n" \
  "ds_read_b64_tr_b16 v[28:29], v4 offset:46112\n" \
  "v_mfma_f32_16x16x32_bf16 v[68:71], v[12:15], a[44:47], v[68:71]\n" \
  "ds_read_b64_tr_b16 v[30:31], v4 offset:47392\n" \
  "s_waitcnt lgkmcnt(7)\n" \
  "v_mfma_f32_16x16x32_bf16 v[52:55], v[16:19], a[40:43], v[52:55]\n" \
  "ds_read_b64_tr_b16 v[12:13], v4 offset:56320\n" \
  "v_mfma_f32_16x16x32_bf16 v[72:75], v[16:19], a[44:47], v[72:75]\n" \
  "ds_read_b64_tr_b16 v[14:15], v4 offset:57600\n" \
  "s_waitcnt lgkmcnt(6)\n" \
  "v_mfma_f32_16x16x32_bf16 v[56:59], v[20:23], a[40:43], v[56:59]\n" \
  "ds_read_b64_tr_b16 v[16:17], v4 offset:56328\n" \
  "v_mfma_f32_16x16x32_bf16 v[76:79], v[20:23], a[44:47], v[76:79]\n" \
  "ds_read_b64_tr_b16 v[18:19], v4 offset:57608\n" \
  "s_waitcnt lgkmcnt(6)\n" \
  "v_mfma_f32_16x16x32_bf16 v[60:63], v[24:27], a[40:43], v[60:63]\n" \
  "ds_read_b64_tr_b16 v[20:21], v4 offset:56336\n" \
  "v_cmp_gt_u32_e32 vcc, 1, v11\n" \
  "v_mfma_f32_16x16x32_bf16 v[80:83], v[24:27], a[44:47], v[80:83]\n" \
  "ds_read_b64_tr_b16 v[22:23], v4 offset:57616\n" \
  "s_waitcnt lgkmcnt(6)\n" \
  "v_mfma_f32_16x16x32_bf16 v[64:67], v[28:31], a[40:43], v[64:67]\n" \
  "ds_read_b64_tr_b16 v[24:25], v4 offset:56344\n" \
  "s_cbranch_vccnz LK3_s\n" \
  "LK3_k:\n" \
  "v_mfma_f32_16x16x32_bf16 v[84:87], v[28:31], a[44:47], v[84:87]\n" \
  "ds_read_b64_tr_b16 v[26:27], v4 offset:57624\n" \
  "s_waitcnt lgkmcnt(6)\n" \
  "v_mfma_f32_16x16x32_bf16 v[88:91], v[12:15], a[40:43], v[88:91]\n" \
  "ds_read_b64_tr_b16 v[28:29], v4 offset:56352\n" \
  "ds_read_b128 a[48:51], v6 offset:12288\n" \
  "v_mfma_f32_16x16x32_bf16 v[108:111], v[12:15], a[44:47], v[108:111]\n" \
  "ds_read_b64_tr_b16 v[30:31], v4 offset:57632\n" \
  "s_waitcnt lgkmcnt(7)\n" \
  "v_mfma_f32_16x16x32_bf16 v[92:95], v[16:19], a[40:43], v[92:95]\n" \
  "ds_read_b64_tr_b16 v[12:13], v5 offset:0\n" \
  "ds_read_b128 a[52:55], v6 offset:14336\n" \
  "v_mfma_f32_16x16x32_bf16 v[112:115], v[16:19], a[44:47], v[112:115]\n" \
  "ds_read_b64_tr_b16 v[14:15], v5 offset:1280\n" \
  "s_waitcnt lgkmcnt(8)\n" \
  "v_mfma_f32_16x16x32_bf16 v[96:99], v[20:23], a[40:43], v[96:99]\n" \
  "ds_read_b64_tr_b16 v[16:17], v5 offset:8\n" \
  "ds_read_b128 a[56:59], v7 offset:12288\n" \
  "v_mfma_f32_16x16x32_bf16 v[116:119], v[20:23], a[44:47], v[116:119]\n" \
  "ds_read_b64_tr_b16 v[18:19], v5 offset:1288\n" \
  "s_waitcnt lgkmcnt(9)\n" \
  "v_mfma_f32_16x16x32_bf16 v[100:103], v[24:27], a[40:43], v[100:103]\n" \
  "ds_read_b64_tr_b16 v[20:21], v5 offset:16\n" \
  "ds_read_b128 a[60:63], v7 offset:14336\n" \
  "v_mfma_f32_16x16x32_bf16 v[120:123], v[24:27], a[44:47], v[120:123]\n" \
  "ds_read_b64_tr_b16 v[22:23], v5 offset:1296\n" \
  "s_waitcnt lgkmcnt(9)\n" \
  "v_mfma_f32_16x16x32_bf16 v[104:107], v[28:31], a[40:43], v[104:107]\n" \
  "ds_read_b64_tr_b16 v[24:25], v5 offset:24\n" \
  "v_mfma_f32_16x16x32_bf16 v[124:127], v[28:31], a[44:47], v[124:127]\n" \
  "ds_read_b64_tr_b16 v[26:27], v5 offset:1304\n" \
  "s_waitcnt lgkmcnt(8)\n" \
  "v_mfma_f32_16x16x32_bf16 v[48:51], v[12:15], a[48:51], v[48:51]\n" \
  "ds_read_b64_tr_b16 v[28:29], v5 offset:32\n" \
  "ds_add_u32 v3, v2\n" \
  "v_mfma_f32_16x16x32_bf16 v[68:71], v[12:15], a[52:55], v[68:71]\n" \
  "ds_read_b64_tr_b16 v[30:31], v5 offset:1312\n" \
  "s_waitcnt lgkmcnt(8)\n" \
  "v_mfma_f32_16x16x32_bf16 v[52:55], v[16:19], a[48:51], v[52:55]\n" \
  "ds_read_b64_tr_b16 v[12:13], v5 offset:10240\n" \
  TSx(30) \
  "v_mfma_f32_16x16x32_bf16 v[72:75], v[16:19], a[52:55], v[72:75]\n" \
  "ds_read_b64_tr_b16 v[14:15], v5 offset:11520\n" \
  "s_waitcnt lgkmcnt(7)\n" \
  "v_mfma_f32_16x16x32_bf16 v[56:59], v[20:23], a[48:51], v[56:59]\n" \
  "ds_read_b64_tr_b16 v[16:17], v5 offset:10248\n" \
  "v_mfma_f32_16x16x32_bf16 v[76:79], v[20:23], a[52:55], v[76:79]\n" \
  "ds_read_b64_tr_b16 v[18:19], v5 offset:11528\n" \
  "s_waitcnt lgkmcnt(7)\n" \
  "v_mfma_f32_16x16x32_bf16 v[60:63], v[24:27], a[48:51], v[60:63]\n" \
  "ds_read_b64_tr_b16 v[20:21], v5 offset:10256\n" \
  "v_mfma_f32_16x16x32_bf16 v[80:83], v[24:27], a[52:55], v[80:83]\n" \
  "ds_read_b64_tr_b16 v[22:23], v5 offset:11536\n" \
  "s_waitcnt lgkmcnt(6)\n" \
  "v_mfma_f32_16x16x32_bf16 v[64:67], v[28:31], a[48:51], v[64:67]\n" \
  "ds_read_b64_tr_b16 v[24:25], v5 offset:10264\n" \
  "s_waitcnt vmcnt(0)\n" \
  "v_mfma_f32_16x16x32_bf16 v[84:87], v[28:31], a[52:55], v[84:87]\n" \
  "ds_read_b64_tr_b16 v[26:27], v5 offset:11544\n" \
  "s_waitcnt lgkmcnt(6)\n" \
  "v_mfma_f32_16x16x32_bf16 v[88:91], v[12:15], a[48:51], v[88:91]\n" \
  "ds_read_b64_tr_b16 v[28:29], v5 offset:10272\n" \
  "v_mfma_f32_16x16x32_bf16 v[108:111], v[12:15], a[52:55], v[108:111]\n" \
  "ds_read_b64_tr_b16 v[30:31], v5 offset:11552\n" \
  "s_waitcnt lgkmcnt(6)\n" \
  "v_mfma_f32_16x16x32_bf16 v[92:95], v[16:19], a[48:51], v[92:95]\n" \
  "ds_read_b64_tr_b16 v[12:13], v5 offset:5120\n" \
  "v_mfma_f32_16x16x32_bf16 v[112:115], v[16:19], a[52:55], v[112:115]\n" \
  "ds_read_b64_tr_b16 v[14:15], v5 offset:6400\n" \
  "s_waitcnt lgkmcnt(6)\n" \
  "v_mfma_f32_16x16x32_bf16 v[96:99], v[20:23], a[48:51], v[96:99]\n" \
  "ds_read_b64_tr_b16 v[16:17], v5 offset:5128\n" \
  "v_mfma_f32_16x16x32_bf16 v[116:119], v[20:23], a[52:55], v[116:119]\n" \
  "ds_read_b64_tr_b16 v[18:19], v5 offset:6408\n" \
  "s_waitcnt lgkmcnt(6)\n" \
  "v_mfma_f32_16x16x32_bf16 v[100:103], v[24:27], a[48:51], v[100:103]\n" \
  "ds_read_b64_tr_b16 v[20:21], v5 offset:5136\n" \
  "ds_read_b32 v11, v8 offset:16\n" \
  "v_mfma_f32_16x16x32_bf16 v[120:123], v[24:27], a[52:55], v[120:123]\n" \
  "ds_read_b64_tr_b16 v[22:23], v5 offset:6416\n" \
  "s_waitcnt lgkmcnt(7)\n" \
  "v_mfma_f32_16x16x32_bf16 v[104:107], v[28:31], a[48:51], v[104:107]\n" \
  "ds_read_b64_tr_b16 v[24:25], v5 offset:5144\n" \
  "v_mfma_f32_16x16x32_bf16 v[124:127], v[28:31], a[52:55], v[124:127]\n" \
  "ds_read_b64_tr_b16 v[26:27], v5 offset:6424\n" \
  "s_waitcnt lgkmcnt(7)\n" \
  "v_mfma_f32_16x16x32_bf16 v[48:51], v[12:15], a[56:59], v[48:51]\n" \
  "ds_read_b64_tr_b16 v[28:29], v5 offset:5152\n" \
  "v_mfma_f32_16x16x32_bf16 v[68:71], v[12:15], a[60:63], v[68:71]\n" \
  "ds_read_b64_tr_b16 v[30:31], v5 offset:6432\n" \
  "s_waitcnt lgkmcnt(7)\n" \
  "v_mfma_f32_16x16x32_bf16 v[52:55], v[16:19], a[56:59], v[52:55]\n" \
  "ds_read_b64_tr_b16 v[12:13], v5 offset:15360\n" \
  "v_mfma_f32_16x16x32_bf16 v[72:75], v[16:19], a[60:63], v[72:75]\n" \
  "ds_read_b64_tr_b16 v[14:15], v5 offset:16640\n" \
  "s_waitcnt lgkmcnt(6)\n" \
  "v_mfma_f32_16x16x32_bf16 v[56:59], v[20:23], a[56:59], v[56:59]\n" \
  "ds_read_b64_tr_b16 v[16:17], v5 offset:15368\n" \
  "v_mfma_f32_16x16x32_bf16 v[76:79], v[20:23], a[60:63], v[76:79]\n" \
  "ds_read_b64_tr_b16 v[18:19], v5 offset:16648\n" \
  "s_waitcnt lgkmcnt(6)\n" \
  "v_mfma_f32_16x16x32_bf16 v[60:63], v[24:27], a[56:59], v[60:63]\n" \
  "ds_read_b64_tr_b16 v[20:21], v5 offset:15376\n" \
  "v_cmp_gt_u32_e32 vcc, 1, v11\n" \
  "v_mfma_f32_16x16x32_bf16 v[80:83], v[24:27], a[60:63], v[80:83]\n" \
  "ds_read_b64_tr_b16 v[22:23], v5 offset:16656\n" \
  "s_waitcnt lgkmcnt(6)\n" \
  "v_mfma_f32_16x16x32_bf16 v[64:67], v[28:31], a[56:59], v[64:67]\n" \
  "ds_read_b64_tr_b16 v[24:25], v5 offset:15384\n" \
  "s_cbranch_vccnz LK4_s\n" \
  "LK4_k:\n" \
  "v_mfma_f32_16x16x32_bf16 v[84:87], v[28:31], a[60:63], v[84:87]\n" \
  "ds_read_b64_tr_b16 v[26:27], v5 offset:16664\n" \
  "s_waitcnt lgkmcnt(6)\n" \
  "v_mfma_f32_16x16x32_bf16 v[88:91], v[12:15], a[56:59], v[88:91]\n" \
  "ds_read_b64_tr_b16 v[28:29], v5 offset:15392\n" \
  "ds_read_b128 a[64:67], v6 offset:0\n" \
  "v_mfma_f32_16x16x32_bf16 v[108:111], v[12:15], a[60:63], v[108:111]\n" \
  "ds_read_b64_tr_b16 v[30:31], v5 offset:16672\n" \
  "s_waitcnt lgkmcnt(7)\n" \
  "v_mfma_f32_16x16x32_bf16 v[92:95], v[16:19], a[56:59], v[92:95]\n" \
  "ds_read_b64_tr_b16 v[12:13], v4 offset:0\n" \
  "ds_read_b128 a[68:71], v6 offset:2048\n" \
  "v_mfma_f32_16x16x32_bf16 v[112:115], v[16:19], a[60:63], v[112:115]\n" \
  "ds_read_b64_tr_b16 v[14:15], v4 offset:1280\n" \
  "s_waitcnt lgkmcnt(8)\n" \
  "v_mfma_f32_16x16x32_bf16 v[96:99], v[20:23], a[56:59], v[96:99]\n" \
  "ds_read_b64_tr_b16 v[16:17], v4 offset:8\n" \
  "ds_read_b128 a[72:75], v7 offset:0\n" \
  "v_mfma_f32_16x16x32_bf16 v[116:119], v[20:23], a[60:63], v[116:119]\n" \
  "ds_read_b64_tr_b16 v[18:19], v4 offset:1288\n" \
  "s_waitcnt lgkmcnt(9)\n" \
  "v_mfma_f32_16x16x32_bf16 v[100:103], v[24:27], a[56:59], v[100:103]\n" \
  "ds_read_b64_tr_b16 v[20:21], v4 offset:16\n" \
  "ds_read_b128 a[76:79], v7 offset:2048\n" \
  "v_mfma_f32_16x16x32_bf16 v[120:123], v[24:27], a[60:63], v[120:123]\n" \
  "ds_read_b64_tr_b16 v[22:23], v4 offset:1296\n" \
  "s_waitcnt lgkmcnt(9)\n" \
  "v_mfma_f32_16x16x32_bf16 v[104:107], v[28:31], a[56:59], v[104:107]\n" \
  "ds_read_b64_tr_b16 v[24:25], v4 offset:24\n" \
  "v_mfma_f32_16x16x32_bf16 v[124:127], v[28:31], a[60:63], v[124:127]\n" \
  "ds_read_b64_tr_b16 v[26:27], v4 offset:1304\n" \
  "s_waitcnt lgkmcnt(8)\n" \
  "v_mfma_f32_16x16x32_bf16 v[48:51], v[12:15], a[64:67], v[48:51]\n" \
  "ds_read_b64_tr_b16 v[28:29], v4 offset:32\n" \
  "ds_add_u32 v3, v2\n" \
  "v_mfma_f32_16x16x32_bf16 v[68:71], v[12:15], a[68:71], v[68:71]\n" \
  "ds_read_b64_tr_b16 v[30:31], v4 offset:1312\n" \
  "s_waitcnt lgkmcnt(8)\n" \
  "v_mfma_f32_16x16x32_bf16 v[52:55], v[16:19], a[64:67], v[52:55]\n" \
  "ds_read_b64_tr_b16 v[12:13], v4 offset:10240\n" \
  TSx(32) \
  "v_mfma_f32_16x16x32_bf16 v[72:75], v[16:19], a[68:71], v[72:75]\n" \
  "ds_read_b64_tr_b16 v[14:15], v4 offset:11520\n" \
  "s_waitcnt lgkmcnt(7)\n" \
  "v_mfma_f32_16x16x32_bf16 v[56:59], v[20:23], a[64:67], v[56:59]\n" \
  "ds_read_b64_tr_b16 v[16:17], v4 offset:10248\n" \
  "v_mfma_f32_16x16x32_bf16 v[76:79], v[20:23], a[68:71], v[76:79]\n" \
  "ds_read_b64_tr_b16 v[18:19], v4 offset:11528\n" \
  "s_waitcnt lgkmcnt(7)\n" \
  "v_mfma_f32_16x16x32_bf16 v[60:63], v[24:27], a[64:67], v[60:63]\n" \
  "ds_read_b64_tr_b16 v[20:21], v4 offset:10256\n" \
  "v_mfma_f32_16x16x32_bf16 v[80:83], v[24:27], a[68:71], v[80:83]\n" \
  "ds_read_b64_tr_b16 v[22:23], v4 offset:11536\n" \
  "s_waitcnt lgkmcnt(6)\n" \
  "v_mfma_f32_16x16x32_bf16 v[64:67], v[28:31], a[64:67], v[64:67]\n" \
  "ds_read_b64_tr_b16 v[24:25], v4 offset:10264\n" \
  "v_mfma_f32_16x16x32_bf16 v[84:87], v[28:31], a[68:71], v[84:87]\n" \
  "ds_read_b64_tr_b16 v[26:27], v4 offset:11544\n" \
  "s_waitcnt lgkmcnt(6)\n" \
  "v_mfma_f32_16x16x32_bf16 v[88:91], v[12:15], a[64:67], v[88:91]\n" \
  "ds_read_b64_tr_b16 v[28:29], v4 offset:10272\n" \
  "v_mfma_f32_16x16x32_bf16 v[108:111], v[12:15], a[68:71], v[108:111]\n" \
  "ds_read_b64_tr_b16 v[30:31], v4 offset:11552\n" \
  "s_waitcnt lgkmcnt(6)\n" \
  "v_mfma_f32_16x16x32_bf16 v[92:95], v[16:19], a[64:67], v[92:95]\n" \
  "ds_read_b64_tr_b16 v[12:13], v4 offset:5120\n" \
  "v_mfma_f32_16x16x32_bf16 v[112:115], v[16:19], a[68:71], v[112:115]\n" \
  "ds_read_b64_tr_b16 v[14:15], v4 offset:6400\n" \
  "s_waitcnt lgkmcnt(6)\n" \
  "v_mfma_f32_16x16x32_bf16 v[96:99], v[20:23], a[64:67], v[96:99]\n" \
  "ds_read_b64_tr_b16 v[16:17], v4 offset:5128\n" \
  "v_mfma_f32_16x16x32_bf16 v[116:119], v[20:23], a[68:71], v[116:119]\n" \
  "ds_read_b64_tr_b16 v[18:19], v4 offset:6408\n" \
  "s_waitcnt lgkmcnt(6)\n" \
  "v_mfma_f32_16x16x32_bf16 v[100:103], v[24:27], a[64:67], v[100:103]\n" \
  "ds_read_b64_tr_b16 v[20:21], v4 offset:5136\n" \
  "ds_read_b32 v11, v8 offset:20\n" \
  "v_mfma_f32_16x16x32_bf16 v[120:123], v[24:27], a[68:71], v[120:123]\n" \
  "ds_read_b64_tr_b16 v[22:23], v4 offset:6416\n" \
  "s_waitcnt lgkmcnt(7)\n" \
  "v_mfma_f32_16x16x32_bf16 v[104:107], v[28:31], a[64:67], v[104:107]\n" \
  "ds_read_b64_tr_b16 v[24:25], v4 offset:5144\n" \
  "v_mfma_f32_16x16x32_bf16 v[124:127], v[28:31], a[68:71], v[124:127]\n" \
  "ds_read_b64_tr_b16 v[26:27], v4 offset:6424\n" \
  "s_waitcnt lgkmcnt(7)\n" \
  "v_mfma_f32_16x16x32_bf16 v[48:51], v[12:15], a[72:75], v[48:51]\n" \
  "ds_read_b64_tr_b16 v[28:29], v4 offset:5152\n" \
  "v_mfma_f32_16x16x32_bf16 v[68:71], v[12:15], a[76:79], v[68:71]\n" \
  "ds_read_b64_tr_b16 v[30:31], v4 offset:6432\n" \
  "s_waitcnt lgkmcnt(7)\n" \
  "v_mfma_f32_16x16x32_bf16 v[52:55], v[16:19], a[72:75], v[52:55]\n" \
  "ds_read_b64_tr_b16 v[12:13], v4 offset:15360\n" \
  "v_mfma_f32_16x16x32_bf16 v[72:75], v[16:19], a[76:79], v[72:75]\n" \
  "ds_read_b64_tr_b16 v[14:15], v4 offset:16640\n" \
  "s_waitcnt lgkmcnt(6)\n" \
  "v_mfma_f32_16x16x32_bf16 v[56:59], v[20:23], a[72:75], v[56:59]\n" \
  "ds_read_b64_tr_b16 v[16:17], v4 offset:15368\n" \
  "v_mfma_f32_16x16x32_bf16 v[76:79], v[20:23], a[76:79], v[76:79]\n" \
  "ds_read_b64_tr_b16 v[18:19], v4 offset:16648\n" \
  "s_waitcnt lgkmcnt(6)\n" \
  "v_mfma_f32_16x16x32_bf16 v[60:63], v[24:27], a[72:75], v[60:63]\n" \
  "ds_read_b64_tr_b16 v[20:21], v4 offset:15376\n" \
  "v_cmp_gt_u32_e32 vcc, 1, v11\n" \
  "v_mfma_f32_16x16x32_bf16 v[80:83], v[24:27], a[76:79], v[80:83]\n" \
  "ds_read_b64_tr_b16 v[22:23], v4 offset:16656\n" \
  "s_waitcnt lgkmcnt(6)\n" \
  "v_mfma_f32_16x16x32_bf16 v[64:67], v[28:31], a[72:75], v[64:67]\n" \
  "ds_read_b64_tr_b16 v[24:25], v4 offset:15384\n" \
  "s_cbranch_vccnz LK5_s\n" \
  "LK5_k:\n" \
  "v_mfma_f32_16x16x32_bf16 v[84:87], v[28:31], a[76:79], v[84:87]\n" \
  "ds_read_b64_tr_b16 v[26:27], v4 offset:16664\n" \
  "s_waitcnt lgkmcnt(6)\n" \
  "v_mfma_f32_16x16x32_bf16 v[88:91], v[12:15], a[72:75], v[88:91]\n" \
  "ds_read_b64_tr_b16 v[28:29], v4 offset:15392\n" \
  "ds_read_b128 a[80:83], v6 offset:4096\n" \
  "v_mfma_f32_16x16x32_bf16 v[108:111], v[12:15], a[76:79], v[108:111]\n" \
  "ds_read_b64_tr_b16 v[30:31], v4 offset:16672\n" \
  "s_waitcnt lgkmcnt(7)\n" \
  "v_mfma_f32_16x16x32_bf16 v[92:95], v[16:19], a[72:75], v[92:95]\n" \
  "ds_read_b64_tr_b16 v[12:13], v4 offset:20480\n" \
  "ds_read_b128 a[84:87], v6 offset:6144\n" \
  "v_mfma_f32_16x16x32_bf16 v[112:115], v[16:19], a[76:79], v[112:115]\n" \
  "ds_read_b64_tr_b16 v[14:15], v4 offset:21760\n" \
  "s_waitcnt lgkmcnt(8)\n" \
  "v_mfma_f32_16x16x32_bf16 v[96:99], v[20:23], a[72:75], v[96:99]\n" \
  "ds_read_b64_tr_b16 v[16:17], v4 offset:20488\n" \
  "ds_read_b128 a[88:91], v7 offset:4096\n" \
  "v_mfma_f32_16x16x32_bf16 v[116:119], v[20:23], a[76:79], v[116:119]\n" \
  "ds_read_b64_tr_b16 v[18:19], v4 offset:21768\n" \
  "s_waitcnt lgkmcnt(9)\n" \
  "v_mfma_f32_16x16x32_bf16 v[100:103], v[24:27], a[72:75], v[100:103]\n" \
  "ds_read_b64_tr_b16 v[20:21], v4 offset:20496\n" \
  "ds_read_b128 a[92:95], v7 offset:6144\n" \
  "v_mfma_f32_16x16x32_bf16 v[120:123], v[24:27], a[76:79], v[120:123]\n" \
  "ds_read_b64_tr_b16 v[22:23], v4 offset:21776\n" \
  "s_waitcnt lgkmcnt(9)\n" \
  "v_mfma_f32_16x16x32_bf16 v[104:107], v[28:31], a[72:75], v[104:107]\n" \
  "ds_read_b64_tr_b16 v[24:25], v4 offset:20504\n" \
  "v_mfma_f32_16x16x32_bf16 v[124:127], v[28:31], a[76:79], v[124:127]\n" \
  "ds_read_b64_tr_b16 v[26:27], v4 offset:21784\n" \
  "s_waitcnt lgkmcnt(8)\n" \
  "v_mfma_f32_16x16x32_bf16 v[48:51], v[12:15], a[80:83], v[48:51]\n" \
  "ds_read_b64_tr_b16 v[28:29], v4 offset:20512\n" \
  "ds_add_u32 v3, v2\n" \
  "v_mfma_f32_16x16x32_bf16 v[68:71], v[12:15], a[84:87], v[68:71]\n" \
  "ds_read_b64_tr_b16 v[30:31], v4 offset:21792\n" \
  "s_waitcnt lgkmcnt(8)\n" \
  "v_mfma_f32_16x16x32_bf16 v[52:55], v[16:19], a[80:83], v[52:55]\n" \
  "ds_read_b64_tr_b16 v[12:13], v4 offset:30720\n" \
  TSx(34) \
  "v_mfma_f32_16x16x32_bf16 v[72:75], v[16:19], a[84:87], v[72:75]\n" \
  "ds_read_b64_tr_b16 v[14:15], v4 offset:32000\n" \
  "s_waitcnt lgkmcnt(7)\n" \
  "v_mfma_f32_16x16x32_bf16 v[56:59], v[20:23], a[80:83], v[56:59]\n" \
  "ds_read_b64_tr_b16 v[16:17], v4 offset:30728\n" \
  "v_mfma_f32_16x16x32_bf16 v[76:79], v[20:23], a[84:87], v[76:79]\n" \
  "ds_read_b64_tr_b16 v[18:19], v4 offset:32008\n" \
  "s_waitcnt lgkmcnt(7)\n" \
  "v_mfma_f32_16x16x32_bf16 v[60:63], v[24:27], a[80:83], v[60:63]\n" \
  "ds_read_b64_tr_b16 v[20:21], v4 offset:30736\n" \
  "v_mfma_f32_16x16x32_bf16 v[80:83], v[24:27], a[84:87], v[80:83]\n" \
  "ds_read_b64_tr_b16 v[22:23], v4 offset:32016\n" \
  "s_waitcnt lgkmcnt(6)\n" \
  "v_mfma_f32_16x16x32_bf16 v[64:67], v[28:31], a[80:83], v[64:67]\n" \
  "ds_read_b64_tr_b16 v[24:25], v4 offset:30744\n" \
  "v_mfma_f32_16x16x32_bf16 v[84:87], v[28:31], a[84:87], v[84:87]\n" \
  "ds_read_b64_tr_b16 v[26:27], v4 offset:32024\n" \
  "s_waitcnt lgkmcnt(6)\n" \
  "v_mfma_f32_16x16x32_bf16 v[88:91], v[12:15], a[80:83], v[88:91]\n" \
  "ds_read_b64_tr_b16 v[28:29], v4 offset:30752\n" \
  "v_mfma_f32_16x16x32_bf16 v[108:111], v[12:15], a[84:87], v[108:111]\n" \
  "ds_read_b64_tr_b16 v[30:31], v4 offset:32032\n" \
  "s_waitcnt lgkmcnt(6)\n" \
  "v_mfma_f32_16x16x32_bf16 v[92:95], v[16:19], a[80:83], v[92:95]\n" \
  "ds_read_b64_tr_b16 v[12:13], v4 offset:25600\n" \
  "v_mfma_f32_16x16x32_bf16 v[112:115], v[16:19], a[84:87], v[112:115]\n" \
  "ds_read_b64_tr_b16 v[14:15], v4 offset:26880\n" \
  "s_waitcnt lgkmcnt(6)\n" \
  "v_mfma_f32_16x16x32_bf16 v[96:99], v[20:23], a[80:83], v[96:99]\n" \
  "ds_read_b64_tr_b16 v[16:17], v4 offset:25608\n" \
  "v_mfma_f32_16x16x32_bf16 v[116:119], v[20:23], a[84:87], v[116:119]\n" \
  "ds_read_b64_tr_b16 v[18:19], v4 offset:26888\n" \
  "s_waitcnt lgkmcnt(6)\n" \
  "v_mfma_f32_16x16x32_bf16 v[100:103], v[24:27], a[80:83], v[100:103]\n" \
  "ds_read_b64_tr_b16 v[20:21], v4 offset:25616\n" \
  "ds_read_b32 v11, v8 offset:24\n" \
  "v_mfma_f32_16x16x32_bf16 v[120:123], v[24:27], a[84:87], v[120:123]\n" \
  "ds_read_b64_tr_b16 v[22:23], v4 offset:26896\n" \
  "s_waitcnt lgkmcnt(7)\n" \
  "v_mfma_f32_16x16x32_bf16 v[104:107], v[28:31], a[80:83], v[104:107]\n" \
  "ds_read_b64_tr_b16 v[24:25], v4 offset:25624\n" \
  "v_mfma_f32_16x16x32_bf16 v[124:127], v[28:31], a[84:87], v[124:127]\n" \
  "ds_read_b64_tr_b16 v[26:27], v4 offset:26904\n" \
  "s_waitcnt lgkmcnt(7)\n" \
  "v_mfma_f32_16x16x32_bf16 v[48:51], v[12:15], a[88:91], v[48:51]\n" \
  "ds_read_b64_tr_b16 v[28:29], v4 offset:25632\n" \
  "v_mfma_f32_16x16x32_bf16 v[68:71], v[12:15], a[92:95], v[68:71]\n" \
  "ds_read_b64_tr_b16 v[30:31], v4 offset:26912\n" \
  "s_waitcnt lgkmcnt(7)\n" \
  "v_mfma_f32_16x16x32_bf16 v[52:55], v[16:19], a[88:91], v[52:55]\n" \
  "ds_read_b64_tr_b16 v[12:13], v4 offset:35840\n" \
  "v_mfma_f32_16x16x32_bf16 v[72:75], v[16:19], a[92:95], v[72:75]\n" \
  "ds_read_b64_tr_b16 v[14:15], v4 offset:37120\n" \
  "s_waitcnt lgkmcnt(6)\n" \
  "v_mfma_f32_16x16x32_bf16 v[56:59], v[20:23], a[88:91], v[56:59]\n" \
  "ds_read_b64_tr_b16 v[16:17], v4 offset:35848\n" \
  "v_mfma_f32_16x16x32_bf16 v[76:79], v[20:23], a[92:95], v[76:79]\n" \
  "ds_read_b64_tr_b16 v[18:19], v4 offset:37128\n" \
  "s_waitcnt lgkmcnt(6)\n" \
  "v_mfma_f32_16x16x32_bf16 v[60:63], v[24:27], a[88:91], v[60:63]\n" \
  "ds_read_b64_tr_b16 v[20:21], v4 offset:35856\n" \
  "v_cmp_gt_u32_e32 vcc, 1, v11\n" \
  "v_mfma_f32_16x16x32_bf16 v[80:83], v[24:27], a[92:95], v[80:83]\n" \
  "ds_read_b64_tr_b16 v[22:23], v4 offset:37136\n" \
  "s_waitcnt lgkmcnt(6)\n" \
  "v_mfma_f32_16x16x32_bf16 v[64:67], v[28:31], a[88:91], v[64:67]\n" \
  "ds_read_b64_tr_b16 v[24:25], v4 offset:35864\n" \
  "s_cbranch_vccnz LK6_s\n" \
  "LK6_k:\n" \
  "v_mfma_f32_16x16x32_bf16 v[84:87], v[28:31], a[92:95], v[84:87]\n" \
  "ds_read_b64_tr_b16 v[26:27], v4 offset:37144\n" \
  "s_waitcnt lgkmcnt(6)\n" \
  "v_mfma_f32_16x16x32_bf16 v[88:91], v[12:15], a[88:91], v[88:91]\n" \
  "ds_read_b64_tr_b16 v[28:29], v4 offset:35872\n" \
  "ds_read_b128 a[96:99], v6 offset:8192\n" \
  "v_mfma_f32_16x16x32_bf16 v[108:111], v[12:15], a[92:95], v[108:111]\n" \
  "ds_read_b64_tr_b16 v[30:31], v4 offset:37152\n" \
  "s_waitcnt lgkmcnt(7)\n" \
  "v_mfma_f32_16x16x32_bf16 v[92:95], v[16:19], a[88:91], v[92:95]\n" \
  "ds_read_b64_tr_b16 v[12:13], v4 offset:40960\n" \
  "ds_read_b128 a[100:103], v6 offset:10240\n" \
  "v_mfma_f32_16x16x32_bf16 v[112:115], v[16:19], a[92:95], v[112:115]\n" \
  "ds_read_b64_tr_b16 v[14:15], v4 offset:42240\n" \
  "s_waitcnt lgkmcnt(8)\n" \
  "v_mfma_f32_16x16x32_bf16 v[96:99], v[20:23], a[88:91], v[96:99]\n" \
  "ds_read_b64_tr_b16 v[16:17], v4 offset:40968\n" \
  "ds_read_b128 a[104:107], v7 offset:8192\n" \
  "v_mfma_f32_16x16x32_bf16 v[116:119], v[20:23], a[92:95], v[116:119]\n" \
  "ds_read_b64_tr_b16 v[18:19], v4 offset:42248\n" \
  "s_waitcnt lgkmcnt(9)\n" \
  "v_mfma_f32_16x16x32_bf16 v[100:103], v[24:27], a[88:91], v[100:103]\n" \
  "ds_read_b64_tr_b16 v[20:21], v4 offset:40976\n" \
  "ds_read_b128 a[108:111], v7 offset:10240\n" \
  "v_mfma_f32_16x16x32_bf16 v[120:123], v[24:27], a[92:95], v[120:123]\n" \
  "ds_read_b64_tr_b16 v[22:23], v4 offset:42256\n" \
  "s_waitcnt lgkmcnt(9)\n" \
  "v_mfma_f32_16x16x32_bf16 v[104:107], v[28:31], a[88:91], v[104:107]\n" \
  "ds_read_b64_tr_b16 v[24:25], v4 offset:40984\n" \
  "v_mfma_f32_16x16x32_bf16 v[124:127], v[28:31], a[92:95], v[124:127]\n" \
  "ds_read_b64_tr_b16 v[26:27], v4 offset:42264\n" \
  "s_waitcnt lgkmcnt(8)\n" \
  "v_mfma_f32_16x16x32_bf16 v[48:51], v[12:15], a[96:99], v[48:51]\n" \
  "ds_read_b64_tr_b16 v[28:29], v4 offset:40992\n" \
  "ds_add_u32 v3, v2\n" \
  "v_mfma_f32_16x16x32_bf16 v[68:71], v[12:15], a[100:103], v[68:71]\n" \
  "ds_read_b64_tr_b16 v[30:31], v4 offset:42272\n" \
  "s_waitcnt lgkmcnt(8)\n" \
  "v_mfma_f32_16x16x32_bf16 v[52:55], v[16:19], a[96:99], v[52:55]\n" \
  "ds_read_b64_tr_b16 v[12:13], v4 offset:51200\n" \
  TSx(36) \
  "v_mfma_f32_16x16x32_bf16 v[72:75], v[16:19], a[100:103], v[72:75]\n" \
  "ds_read_b64_tr_b16 v[14:15], v4 offset:52480\n" \
  "s_waitcnt lgkmcnt(7)\n" \
  "v_mfma_f32_16x16x32_bf16 v[56:59], v[20:23], a[96:99], v[56:59]\n" \
  "ds_read_b64_tr_b16 v[16:17], v4 offset:51208\n" \
  "v_mfma_f32_16x16x32_bf16 v[76:79], v[20:23], a[100:103], v[76:79]\n" \
  "ds_read_b64_tr_b16 v[18:19], v4 offset:52488\n" \
  "s_waitcnt lgkmcnt(7)\n" \
  "v_mfma_f32_16x16x32_bf16 v[60:63], v[24:27], a[96:99], v[60:63]\n" \
  "ds_read_b64_tr_b16 v[20:21], v4 offset:51216\n" \
  "v_mfma_f32_16x16x32_bf16 v[80:83], v[24:27], a[100:103], v[80:83]\n" \
  "ds_read_b64_tr_b16 v[22:23], v4 offset:52496\n" \
  "s_waitcnt lgkmcnt(6)\n" \
  "v_mfma_f32_16x16x32_bf16 v[64:67], v[28:31], a[96:99], v[64:67]\n" \
  "ds_read_b64_tr_b16 v[24:25], v4 offset:51224\n" \
  "v_mfma_f32_16x16x32_bf16 v[84:87], v[28:31], a[100:103], v[84:87]\n" \
  "ds_read_b64_tr_b16 v[26:27], v4 offset:52504\n" \
  "s_waitcnt lgkmcnt(6)\n" \
  "v_mfma_f32_16x16x32_bf16 v[88:91], v[12:15], a[96:99], v[88:91]\n" \
  "ds_read_b64_tr_b16 v[28:29], v4 offset:51232\n" \
  "v_mfma_f32_16x16x32_bf16 v[108:111], v[12:15], a[100:103], v[108:111]\n" \
  "ds_read_b64_tr_b16 v[30:31], v4 offset:52512\n" \
  "s_waitcnt lgkmcnt(6)\n" \
  "v_mfma_f32_16x16x32_bf16 v[92:95], v[16:19], a[96:99], v[92:95]\n" \
  "ds_read_b64_tr_b16 v[12:13], v4 offset:46080\n" \
  "v_mfma_f32_16x16x32_bf16 v[112:115], v[16:19], a[100:103], v[112:115]\n" \
  "ds_read_b64_tr_b16 v[14:15], v4 offset:47360\n" \
  "s_waitcnt lgkmcnt(6)\n" \
  "v_mfma_f32_16x16x32_bf16 v[96:99], v[20:23], a[96:99], v[96:99]\n" \
  "ds_read_b64_tr_b16 v[16:17], v4 offset:46088\n" \
  "v_mfma_f32_16x16x32_bf16 v[116:119], v[20:23], a[100:103], v[116:119]\n" \
  "ds_read_b64_tr_b16 v[18:19], v4 offset:47368\n" \
  "s_waitcnt lgkmcnt(6)\n" \
  "v_mfma_f32_16x16x32_bf16 v[100:103], v[24:27], a[96:99], v[100:103]\n" \
  "ds_read_b64_tr_b16 v[20:21], v4 offset:46096\n" \
  "ds_read_b32 v11, v8 offset:28\n" \
  "v_mfma_f32_16x16x32_bf16 v[120:123], v[24:27], a[100:103], v[120:123]\n" \
  "ds_read_b64_tr_b16 v[22:23], v4 offset:47376\n" \
  "s_waitcnt lgkmcnt(7)\n" \
  "v_mfma_f32_16x16x32_bf16 v[104:107], v[28:31], a[96:99], v[104:107]\n" \
  "ds_read_b64_tr_b16 v[24:25], v4 offset:46104\n" \
  "v_mfma_f32_16x16x32_bf16 v[124:127], v[28:31], a[100:103], v[124:127]\n" \
  "ds_read_b64_tr_b16 v[26:27], v4 offset:47384\n" \
  "s_waitcnt lgkmcnt(7)\n" \
  "v_mfma_f32_16x16x32_bf16 v[48:51], v[12:15], a[104:107], v[48:51]\n" \
  "ds_read_b64_tr_b16 v[28:29], v4 offset:46112\n" \
  "v_mfma_f32_16x16x32_bf16 v[68:71], v[12:15], a[108:111], v[68:71]\n" \
  "ds_read_b64_tr_b16 v[30:31], v4 offset:47392\n" \
  "s_waitcnt lgkmcnt(7)\n" \
  "v_mfma_f32_16x16x32_bf16 v[52:55], v[16:19], a[104:107], v[52:55]\n" \
  "ds_read_b64_tr_b16 v[12:13], v4 offset:56320\n" \
  "v_mfma_f32_16x16x32_bf16 v[72:75], v[16:19], a[108:111], v[72:75]\n" \
  "ds_read_b64_tr_b16 v[14:15], v4 offset:57600\n" \
  "s_waitcnt lgkmcnt(6)\n" \
  "v_mfma_f32_16x16x32_bf16 v[56:59], v[20:23], a[104:107], v[56:59]\n" \
  "ds_read_b64_tr_b16 v[16:17], v4 offset:56328\n" \
  "v_mfma_f32_16x16x32_bf16 v[76:79], v[20:23], a[108:111], v[76:79]\n" \
  "ds_read_b64_tr_b16 v[18:19], v4 offset:57608\n" \
  "s_waitcnt lgkmcnt(6)\n" \
  "v_mfma_f32_16x16x32_bf16 v[60:63], v[24:27], a[104:107], v[60:63]\n" \
  "ds_read_b64_tr_b16 v[20:21], v4 offset:56336\n" \
  "v_cmp_gt_u32_e32 vcc, 1, v11\n" \
  "v_mfma_f32_16x16x32_bf16 v[80:83], v[24:27], a[108:111], v[80:83]\n" \
  "ds_read_b64_tr_b16 v[22:23], v4 offset:57616\n" \
  "s_waitcnt lgkmcnt(6)\n" \
  "v_mfma_f32_16x16x32_bf16 v[64:67], v[28:31], a[104:107], v[64:67]\n" \
  "ds_read_b64_tr_b16 v[24:25], v4 offset:56344\n" \
  "s_cbranch_vccnz LK7_s\n" \
  "LK7_k:\n" \
  "v_mfma_f32_16x16x32_bf16 v[84:87], v[28:31], a[108:111], v[84:87]\n" \
  "ds_read_b64_tr_b16 v[26:27], v4 offset:57624\n" \
  "s_waitcnt lgkmcnt(6)\n" \
  "v_mfma_f32_16x16x32_bf16 v[88:91], v[12:15], a[104:107], v[88:91]\n" \
  "ds_read_b64_tr_b16 v[28:29], v4 offset:56352\n" \
  "ds_read_b128 a[112:115], v6 offset:12288\n" \
  "v_mfma_f32_16x16x32_bf16 v[108:111], v[12:15], a[108:111], v[108:111]\n" \
  "ds_read_b64_tr_b16 v[30:31], v4 offset:57632\n" \
  "s_waitcnt lgkmcnt(7)\n" \
  "v_mfma_f32_16x16x32_bf16 v[92:95], v[16:19], a[104:107], v[92:95]\n" \
  "ds_read_b64_tr_b16 v[12:13], v5 offset:0\n" \
  "ds_read_b128 a[116:119], v6 offset:14336\n" \
  "v_mfma_f32_16x16x32_bf16 v[112:115], v[16:19], a[108:111], v[112:115]\n" \
  "ds_read_b64_tr_b16 v[14:15], v5 offset:1280\n" \
  "s_waitcnt lgkmcnt(8)\n" \
  "v_mfma_f32_16x16x32_bf16 v[96:99], v[20:23], a[104:107], v[96:99]\n" \
  "ds_read_b64_tr_b16 v[16:17], v5 offset:8\n" \
  "ds_read_b128 a[120:123], v7 offset:12288\n" \
  "v_mfma_f32_16x16x32_bf16 v[116:119], v[20:23], a[108:111], v[116:119]\n" \
  "ds_read_b64_tr_b16 v[18:19], v5 offset:1288\n" \
  "s_waitcnt lgkmcnt(9)\n" \
  "v_mfma_f32_16x16x32_bf16 v[100:103], v[24:27], a[104:107], v[100:103]\n" \
  "ds_read_b64_tr_b16 v[20:21], v5 offset:16\n" \
  "ds_read_b128 a[124:127], v7 offset:14336\n" \
  "v_mfma_f32_16x16x32_bf16 v[120:123], v[24:27], a[108:111], v[120:123]\n" \
  "ds_read_b64_tr_b16 v[22:23], v5 offset:1296\n" \
  "s_waitcnt lgkmcnt(9)\n" \
  "v_mfma_f32_16x16x32_bf16 v[104:107], v[28:31], a[104:107], v[104:107]\n" \
  "ds_read_b64_tr_b16 v[24:25], v5 offset:24\n" \
  "v_mfma_f32_16x16x32_bf16 v[124:127], v[28:31], a[108:111], v[124:127]\n" \
  "ds_read_b64_tr_b16 v[26:27], v5 offset:1304\n" \
  "s_waitcnt lgkmcnt(8)\n" \
  "v_mfma_f32_16x16x32_bf16 v[48:51], v[12:15], a[112:115], v[48:51]\n" \
  "ds_read_b64_tr_b16 v[28:29], v5 offset:32\n" \
  "ds_add_u32 v3, v2\n" \
  "v_mfma_f32_16x16x32_bf16 v[68:71], v[12:15], a[116:119], v[68:71]\n" \
  "ds_read_b64_tr_b16 v[30:31], v5 offset:1312\n" \
  "s_waitcnt lgkmcnt(8)\n" \
  "v_mfma_f32_16x16x32_bf16 v[52:55], v[16:19], a[112:115], v[52:55]\n" \
  "ds_read_b64_tr_b16 v[12:13], v5 offset:10240\n" \
  TSx(38) \
  "v_mfma_f32_16x16x32_bf16 v[72:75], v[16:19], a[116:119], v[72:75]\n" \
  "ds_read_b64_tr_b16 v[14:15], v5 offset:11520\n" \
  "s_waitcnt lgkmcnt(7)\n" \
  "v_mfma_f32_16x16x32_bf16 v[56:59], v[20:23], a[112:115], v[56:59]\n" \
  "ds_read_b64_tr_b16 v[16:17], v5 offset:10248\n" \
  "v_mfma_f32_16x16x32_bf16 v[76:79], v[20:23], a[116:119], v[76:79]\n" \
  "ds_read_b64_tr_b16 v[18:19], v5 offset:11528\n" \
  "s_waitcnt lgkmcnt(7)\n" \
  "v_mfma_f32_16x16x32_bf16 v[60:63], v[24:27], a[112:115], v[60:63]\n" \
  "ds_read_b64_tr_b16 v[20:21], v5 offset:10256\n" \
  "v_mfma_f32_16x16x32_bf16 v[80:83], v[24:27], a[116:119], v[80:83]\n" \
  "ds_read_b64_tr_b16 v[22:23], v5 offset:11536\n" \
  "s_waitcnt lgkmcnt(6)\n" \
  "v_mfma_f32_16x16x32_bf16 v[64:67], v[28:31], a[112:115], v[64:67]\n" \
  "ds_read_b64_tr_b16 v[24:25], v5 offset:10264\n" \
  "v_mfma_f32_16x16x32_bf16 v[84:87], v[28:31], a[116:119], v[84:87]\n" \
  "ds_read_b64_tr_b16 v[26:27], v5 offset:11544\n" \
  "s_waitcnt lgkmcnt(6)\n" \
  "v_mfma_f32_16x16x32_bf16 v[88:91], v[12:15], a[112:115], v[88:91]\n" \
  "ds_read_b64_tr_b16 v[28:29], v5 offset:10272\n" \
  "v_mfma_f32_16x16x32_bf16 v[108:111], v[12:15], a[116:119], v[108:111]\n" \
  "ds_read_b64_tr_b16 v[30:31], v5 offset:11552\n" \
  "s_waitcnt lgkmcnt(6)\n" \
  "v_mfma_f32_16x16x32_bf16 v[92:95], v[16:19], a[112:115], v[92:95]\n" \
  "ds_read_b64_tr_b16 v[12:13], v5 offset:5120\n" \
  "v_mfma_f32_16x16x32_bf16 v[112:115], v[16:19], a[116:119], v[112:115]\n" \
  "ds_read_b64_tr_b16 v[14:15], v5 offset:6400\n" \
  "s_waitcnt lgkmcnt(6)\n" \
  "v_mfma_f32_16x16x32_bf16 v[96:99], v[20:23], a[112:115], v[96:99]\n" \
  "ds_read_b64_tr_b16 v[16:17], v5 offset:5128\n" \
  "v_mfma_f32_16x16x32_bf16 v[116:119], v[20:23], a[116:119], v[116:119]\n" \
  "ds_read_b64_tr_b16 v[18:19], v5 offset:6408\n" \
  "s_waitcnt lgkmcnt(6)\n" \
  "v_mfma_f32_16x16x32_bf16 v[100:103], v[24:27], a[112:115], v[100:103]\n" \
  "ds_read_b64_tr_b16 v[20:21], v5 offset:5136\n" \
  "ds_read_b32 v11, v8 offset:32\n" \
  "v_mfma_f32_16x16x32_bf16 v[120:123], v[24:27], a[116:119], v[120:123]\n" \
  "ds_read_b64_tr_b16 v[22:23], v5 offset:6416\n" \
  "s_waitcnt lgkmcnt(7)\n" \
  "v_mfma_f32_16x16x32_bf16 v[104:107], v[28:31], a[112:115], v[104:107]\n" \
  "ds_read_b64_tr_b16 v[24:25], v5 offset:5144\n" \
  "v_mfma_f32_16x16x32_bf16 v[124:127], v[28:31], a[116:119], v[124:127]\n" \
  "ds_read_b64_tr_b16 v[26:27], v5 offset:6424\n" \
  "s_waitcnt lgkmcnt(7)\n" \
  "v_mfma_f32_16x16x32_bf16 v[48:51], v[12:15], a[120:123], v[48:51]\n" \
  "ds_read_b64_tr_b16 v[28:29], v5 offset:5152\n" \
  "v_mfma_f32_16x16x32_bf16 v[68:71], v[12:15], a[124:127], v[68:71]\n" \
  "ds_read_b64_tr_b16 v[30:31], v5 offset:6432\n" \
  "s_waitcnt lgkmcnt(7)\n" \
  "v_mfma_f32_16x16x32_bf16 v[52:55], v[16:19], a[120:123], v[52:55]\n" \
  "ds_read_b64_tr_b16 v[12:13], v5 offset:15360\n" \
  "v_mfma_f32_16x16x32_bf16 v[72:75], v[16:19], a[124:127], v[72:75]\n" \
  "ds_read_b64_tr_b16 v[14:15], v5 offset:16640\n" \
  "s_waitcnt lgkmcnt(6)\n" \
  "v_mfma_f32_16x16x32_bf16 v[56:59], v[20:23], a[120:123], v[56:59]\n" \
  "ds_read_b64_tr_b16 v[16:17], v5 offset:15368\n" \
  "v_mfma_f32_16x16x32_bf16 v[76:79], v[20:23], a[124:127], v[76:79]\n" \
  "ds_read_b64_tr_b16 v[18:19], v5 offset:16648\n" \
  "s_waitcnt lgkmcnt(6)\n" \
  "v_mfma_f32_16x16x32_bf16 v[60:63], v[24:27], a[120:123], v[60:63]\n" \
  "ds_read_b64_tr_b16 v[20:21], v5 offset:15376\n" \
  "v_cmp_gt_u32_e32 vcc, 1, v11\n" \
  "v_mfma_f32_16x16x32_bf16 v[80:83], v[24:27], a[124:127], v[80:83]\n" \
  "ds_read_b64_tr_b16 v[22:23], v5 offset:16656\n" \
  "s_waitcnt lgkmcnt(6)\n" \
  "v_mfma_f32_16x16x32_bf16 v[64:67], v[28:31], a[120:123], v[64:67]\n" \
  "ds_read_b64_tr_b16 v[24:25], v5 offset:15384\n" \
  "s_cbranch_vccnz LK8_s\n" \
  "LK8_k:\n" \
  "v_mfma_f32_16x16x32_bf16 v[84:87], v[28:31], a[124:127], v[84:87]\n" \
  "ds_read_b64_tr_b16 v[26:27], v5 offset:16664\n" \
  "s_waitcnt lgkmcnt(6)\n" \
  "v_mfma_f32_16x16x32_bf16 v[88:91], v[12:15], a[120:123], v[88:91]\n" \
  "ds_read_b64_tr_b16 v[28:29], v5 offset:15392\n" \
  "ds_read_b128 v[32:35], v6 offset:0\n" \
  "v_mfma_f32_16x16x32_bf16 v[108:111], v[12:15], a[124:127], v[108:111]\n" \
  "ds_read_b64_tr_b16 v[30:31], v5 offset:16672\n" \
  "s_waitcnt lgkmcnt(7)\n" \
  "v_mfma_f32_16x16x32_bf16 v[92:95], v[16:19], a[120:123], v[92:95]\n" \
  "ds_read_b64_tr_b16 v[12:13], v4 offset:0\n" \
  "ds_read_b128 v[36:39], v6 offset:2048\n" \
  "v_mfma_f32_16x16x32_bf16 v[112:115], v[16:19], a[124:127], v[112:115]\n" \
  "ds_read_b64_tr_b16 v[14:15], v4 offset:1280\n" \
  "s_waitcnt lgkmcnt(8)\n" \
  "v_mfma_f32_16x16x32_bf16 v[96:99], v[20:23], a[120:123], v[96:99]\n" \
  "ds_read_b64_tr_b16 v[16:17], v4 offset:8\n" \
  "ds_read_b128 v[40:43], v7 offset:0\n" \
  "v_mfma_f32_16x16x32_bf16 v[116:119], v[20:23], a[124:127], v[116:119]\n" \
  "ds_read_b64_tr_b16 v[18:19], v4 offset:1288\n" \
  "s_waitcnt lgkmcnt(9)\n" \
  "v_mfma_f32_16x16x32_bf16 v[100:103], v[24:27], a[120:123], v[100:103]\n" \
  "ds_read_b64_tr_b16 v[20:21], v4 offset:16\n" \
  "ds_read_b128 v[44:47], v7 offset:2048\n" \
  "v_mfma_f32_16x16x32_bf16 v[120:123], v[24:27], a[124:127], v[120:123]\n" \
  "ds_read_b64_tr_b16 v[22:23], v4 offset:1296\n" \
  "s_waitcnt lgkmcnt(9)\n" \
  "v_mfma_f32_16x16x32_bf16 v[104:107], v[28:31], a[120:123], v[104:107]\n" \
  "ds_read_b64_tr_b16 v[24:25], v4 offset:24\n" \
  "v_mfma_f32_16x16x32_bf16 v[124:127], v[28:31], a[124:127], v[124:127]\n" \
  "ds_read_b64_tr_b16 v[26:27], v4 offset:1304\n" \
  "s_waitcnt lgkmcnt(8)\n" \
  "v_mfma_f32_16x16x32_bf16 v[48:51], v[12:15], v[32:35], v[48:51]\n" \
  "ds_read_b64_tr_b16 v[28:29], v4 offset:32\n" \
  "ds_add_u32 v3, v2\n" \
  "v_mfma_f32_16x16x32_bf16 v[68:71], v[12:15], v[36:39], v[68:71]\n" \
  "ds_read_b64_tr_b16 v[30:31], v4 offset:1312\n" \
  "s_waitcnt lgkmcnt(8)\n" \
  "v_mfma_f32_16x16x32_bf16 v[52:55], v[16:19], v[32:35], v[52:55]\n" \
  "ds_read_b64_tr_b16 v[12:13], v4 offset:10240\n" \
  TSx(40) \
  "v_mfma_f32_16x16x32_bf16 v[72:75], v[16:19], v[36:39], v[72:75]\n" \
  "ds_read_b64_tr_b16 v[14:15], v4 offset:11520\n" \
  "s_waitcnt lgkmcnt(7)\n" \
  "v_mfma_f32_16x16x32_bf16 v[56:59], v[20:23], v[32:35], v[56:59]\n" \
  "ds_read_b64_tr_b16 v[16:17], v4 offset:10248\n" \
  "v_mfma_f32_16x16x32_bf16 v[76:79], v[20:23], v[36:39], v[76:79]\n" \
  "ds_read_b64_tr_b16 v[18:19], v4 offset:11528\n" \
  "s_waitcnt lgkmcnt(7)\n" \
  "v_mfma_f32_16x16x32_bf16 v[60:63], v[24:27], v[32:35], v[60:63]\n" \
  "ds_read_b64_tr_b16 v[20:21], v4 offset:10256\n" \
  "v_mfma_f32_16x16x32_bf16 v[80:83], v[24:27], v[36:39], v[80:83]\n" \
  "ds_read_b64_tr_b16 v[22:23], v4 offset:11536\n" \
  "s_waitcnt lgkmcnt(6)\n" \
  "v_mfma_f32_16x16x32_bf16 v[64:67], v[28:31], v[32:35], v[64:67]\n" \
  "ds_read_b64_tr_b16 v[24:25], v4 offset:10264\n" \
  "v_mfma_f32_16x16x32_bf16 v[84:87], v[28:31], v[36:39], v[84:87]\n" \
  "ds_read_b64_tr_b16 v[26:27], v4 offset:11544\n" \
  "s_waitcnt lgkmcnt(6)\n" \
  "v_mfma_f32_16x16x32_bf16 v[88:91], v[12:15], v[32:35], v[88:91]\n" \
  "ds_read_b64_tr_b16 v[28:29], v4 offset:10272\n" \
  "v_mfma_f32_16x16x32_bf16 v[108:111], v[12:15], v[36:39], v[108:111]\n" \
  "ds_read_b64_tr_b16 v[30:31], v4 offset:11552\n" \
  "s_waitcnt lgkmcnt(6)\n" \
  "v_mfma_f32_16x16x32_bf16 v[92:95], v[16:19], v[32:35], v[92:95]\n" \
  "ds_read_b64_tr_b16 v[12:13], v4 offset:5120\n" \
  "v_mfma_f32_16x16x32_bf16 v[112:115], v[16:19], v[36:39], v[112:115]\n" \
  "ds_read_b64_tr_b16 v[14:15], v4 offset:6400\n" \
  "s_waitcnt lgkmcnt(6)\n" \
  "v_mfma_f32_16x16x32_bf16 v[96:99], v[20:23], v[32:35], v[96:99]\n" \
  "ds_read_b64_tr_b16 v[16:17], v4 offset:5128\n" \
  "v_mfma_f32_16x16x32_bf16 v[116:119], v[20:23], v[36:39], v[116:119]\n" \
  "ds_read_b64_tr_b16 v[18:19], v4 offset:6408\n" \
  "s_waitcnt lgkmcnt(6)\n" \
  "v_mfma_f32_16x16x32_bf16 v[100:103], v[24:27], v[32:35], v[100:103]\n" \
  "ds_read_b64_tr_b16 v[20:21], v4 offset:5136\n" \
  "ds_read_b32 v11, v8 offset:36\n" \
  "v_mfma_f32_16x16x32_bf16 v[120:123], v[24:27], v[36:39], v[120:123]\n" \
  "ds_read_b64_tr_b16 v[22:23], v4 offset:6416\n" \
  "s_waitcnt lgkmcnt(7)\n" \
  "v_mfma_f32_16x16x32_bf16 v[104:107], v[28:31], v[32:35], v[104:107]\n" \
  "ds_read_b64_tr_b16 v[24:25], v4 offset:5144\n" \
  "v_mfma_f32_16x16x32_bf16 v[124:127], v[28:31], v[36:39], v[124:127]\n" \
  "ds_read_b64_tr_b16 v[26:27], v4 offset:6424\n" \
  "s_waitcnt lgkmcnt(7)\n" \
  "v_mfma_f32_16x16x32_bf16 v[48:51], v[12:15], v[40:43], v[48:51]\n" \
  "ds_read_b64_tr_b16 v[28:29], v4 offset:5152\n" \
  "v_mfma_f32_16x16x32_bf16 v[68:71], v[12:15], v[44:47], v[68:71]\n" \
  "ds_read_b64_tr_b16 v[30:31], v4 offset:6432\n" \
  "s_waitcnt lgkmcnt(7)\n" \
  "v_mfma_f32_16x16x32_bf16 v[52:55], v[16:19], v[40:43], v[52:55]\n" \
  "ds_read_b64_tr_b16 v[12:13], v4 offset:15360\n" \
  "v_mfma_f32_16x16x32_bf16 v[72:75], v[16:19], v[44:47], v[72:75]\n" \
  "ds_read_b64_tr_b16 v[14:15], v4 offset:16640\n" \
  "s_waitcnt lgkmcnt(6)\n" \
  "v_mfma_f32_16x16x32_bf16 v[56:59], v[20:23], v[40:43], v[56:59]\n" \
  "ds_read_b64_tr_b16 v[16:17], v4 offset:15368\n" \
  "v_mfma_f32_16x16x32_bf16 v[76:79], v[20:23], v[44:47], v[76:79]\n" \
  "ds_read_b64_tr_b16 v[18:19], v4 offset:16648\n" \
  "s_waitcnt lgkmcnt(6)\n" \
  "v_mfma_f32_16x16x32_bf16 v[60:63], v[24:27], v[40:43], v[60:63]\n" \
  "ds_read_b64_tr_b16 v[20:21], v4 offset:15376\n" \
  "v_cmp_gt_u32_e32 vcc, 1, v11\n" \
  "v_mfma_f32_16x16x32_bf16 v[80:83], v[24:27], v[44:47], v[80:83]\n" \
  "ds_read_b64_tr_b16 v[22:23], v4 offset:16656\n" \
  "s_waitcnt lgkmcnt(6)\n" \
  "v_mfma_f32_16x16x32_bf16 v[64:67], v[28:31], v[40:43], v[64:67]\n" \
  "ds_read_b64_tr_b16 v[24:25], v4 offset:15384\n" \
  "s_cbranch_vccnz LK9_s\n" \
  "LK9_k:\n" \
  "v_mfma_f32_16x16x32_bf16 v[84:87], v[28:31], v[44:47], v[84:87]\n" \
  "ds_read_b64_tr_b16 v[26:27], v4 offset:16664\n" \
  "s_waitcnt lgkmcnt(6)\n" \
  "v_mfma_f32_16x16x32_bf16 v[88:91], v[12:15], v[40:43], v[88:91]\n" \
  "ds_read_b64_tr_b16 v[28:29], v4 offset:15392\n" \
  "ds_read_b128 v[32:35], v6 offset:4096\n" \
  "v_mfma_f32_16x16x32_bf16 v[108:111], v[12:15], v[44:47], v[108:111]\n" \
  "ds_read_b64_tr_b16 v[30:31], v4 offset:16672\n" \
  "s_waitcnt lgkmcnt(7)\n" \
  "v_mfma_f32_16x16x32_bf16 v[92:95], v[16:19], v[40:43], v[92:95]\n" \
  "ds_read_b64_tr_b16 v[12:13], v4 offset:30720\n" \
  "ds_read_b128 v[36:39], v6 offset:6144\n" \
  "v_mfma_f32_16x16x32_bf16 v[112:115], v[16:19], v[44:47], v[112:115]\n" \
  "ds_read_b64_tr_b16 v[14:15], v4 offset:32000\n" \
  "s_waitcnt lgkmcnt(8)\n" \
  "v_mfma_f32_16x16x32_bf16 v[96:99], v[20:23], v[40:43], v[96:99]\n" \
  "ds_read_b64_tr_b16 v[16:17], v4 offset:30728\n" \
  "v_mfma_f32_16x16x32_bf16 v[116:119], v[20:23], v[44:47], v[116:119]\n" \
  "ds_read_b64_tr_b16 v[18:19], v4 offset:32008\n" \
  "s_waitcnt lgkmcnt(8)\n" \
  "v_mfma_f32_16x16x32_bf16 v[100:103], v[24:27], v[40:43], v[100:103]\n" \
  "ds_read_b64_tr_b16 v[20:21], v4 offset:30736\n" \
  "v_mfma_f32_16x16x32_bf16 v[120:123], v[24:27], v[44:47], v[120:123]\n" \
  "ds_read_b64_tr_b16 v[22:23], v4 offset:32016\n" \
  "s_waitcnt lgkmcnt(7)\n" \
  "v_mfma_f32_16x16x32_bf16 v[104:107], v[28:31], v[40:43], v[104:107]\n" \
  "ds_read_b64_tr_b16 v[24:25], v4 offset:30744\n" \
  "v_mfma_f32_16x16x32_bf16 v[124:127], v[28:31], v[44:47], v[124:127]\n" \
  "ds_read_b64_tr_b16 v[26:27], v4 offset:32024\n" \
  "s_waitcnt lgkmcnt(6)\n" \
  "v_mfma_f32_16x16x32_bf16 v[88:91], v[12:15], v[32:35], v[88:91]\n" \
  "ds_read_b64_tr_b16 v[28:29], v4 offset:30752\n" \
  "ds_read_b128 v[40:43], v7 offset:4096\n" \
  "v_mfma_f32_16x16x32_bf16 v[108:111], v[12:15], v[36:39], v[108:111]\n" \
  "ds_read_b64_tr_b16 v[30:31], v4 offset:32032\n" \
  "s_waitcnt lgkmcnt(7)\n" \
  "v_mfma_f32_16x16x32_bf16 v[92:95], v[16:19], v[32:35], v[92:95]\n" \
  "ds_read_b64_tr_b16 v[12:13], v4 offset:35840\n" \
  "ds_read_b128 v[44:47], v7 offset:6144\n" \
  "v_mfma_f32_16x16x32_bf16 v[112:115], v[16:19], v[36:39], v[112:115]\n" \
  "ds_read_b64_tr_b16 v[14:15], v4 offset:37120\n" \
  "s_waitcnt lgkmcnt(8)\n" \
  "v_mfma_f32_16x16x32_bf16 v[96:99], v[20:23], v[32:35], v[96:99]\n" \
  "ds_read_b64_tr_b16 v[16:17], v4 offset:35848\n" \
  "ds_add_u32 v3, v2\n" \
  "v_mfma_f32_16x16x32_bf16 v[116:119], v[20:23], v[36:39], v[116:119]\n" \
  "ds_read_b64_tr_b16 v[18:19], v4 offset:37128\n" \
  "s_waitcnt lgkmcnt(9)\n" \
  "v_mfma_f32_16x16x32_bf16 v[100:103], v[24:27], v[32:35], v[100:103]\n" \
  "ds_read_b64_tr_b16 v[20:21], v4 offset:35856\n" \
  TSx(42) \
  "v_mfma_f32_16x16x32_bf16 v[120:123], v[24:27], v[36:39], v[120:123]\n" \
  "ds_read_b64_tr_b16 v[22:23], v4 offset:37136\n" \
  "s_waitcnt lgkmcnt(8)\n" \
  "v_mfma_f32_16x16x32_bf16 v[104:107], v[28:31], v[32:35], v[104:107]\n" \
  "ds_read_b64_tr_b16 v[24:25], v4 offset:35864\n" \
  "v_mfma_f32_16x16x32_bf16 v[124:127], v[28:31], v[36:39], v[124:127]\n" \
  "ds_read_b64_tr_b16 v[26:27], v4 offset:37144\n" \
  "s_waitcnt lgkmcnt(7)\n" \
  "v_mfma_f32_16x16x32_bf16 v[88:91], v[12:15], v[40:43], v[88:91]\n" \
  "ds_read_b64_tr_b16 v[28:29], v4 offset:35872\n" \
  "v_mfma_f32_16x16x32_bf16 v[108:111], v[12:15], v[44:47], v[108:111]\n" \
  "ds_read_b64_tr_b16 v[30:31], v4 offset:37152\n" \
  "s_waitcnt lgkmcnt(6)\n" \
  "v_mfma_f32_16x16x32_bf16 v[92:95], v[16:19], v[40:43], v[92:95]\n" \
  "ds_read_b64_tr_b16 v[12:13], v4 offset:20480\n" \
  "v_mfma_f32_16x16x32_bf16 v[112:115], v[16:19], v[44:47], v[112:115]\n" \
  "ds_read_b64_tr_b16 v[14:15], v4 offset:21760\n" \
  "s_waitcnt lgkmcnt(6)\n" \
  "v_mfma_f32_16x16x32_bf16 v[96:99], v[20:23], v[40:43], v[96:99]\n" \
  "ds_read_b64_tr_b16 v[16:17], v4 offset:20488\n" \
  "v_mfma_f32_16x16x32_bf16 v[116:119], v[20:23], v[44:47], v[116:119]\n" \
  "ds_read_b64_tr_b16 v[18:19], v4 offset:21768\n" \
  "s_waitcnt lgkmcnt(6)\n" \
  "v_mfma_f32_16x16x32_bf16 v[100:103], v[24:27], v[40:43], v[100:103]\n" \
  "ds_read_b64_tr_b16 v[20:21], v4 offset:20496\n" \
  "v_mfma_f32_16x16x32_bf16 v[120:123], v[24:27], v[44:47], v[120:123]\n" \
  "ds_read_b64_tr_b16 v[22:23], v4 offset:21776\n" \
  "s_waitcnt lgkmcnt(6)\n" \
  "v_mfma_f32_16x16x32_bf16 v[104:107], v[28:31], v[40:43], v[104:107]\n" \
  "ds_read_b64_tr_b16 v[24:25], v4 offset:20504\n" \
  "v_mfma_f32_16x16x32_bf16 v[124:127], v[28:31], v[44:47], v[124:127]\n" \
  "ds_read_b64_tr_b16 v[26:27], v4 offset:21784\n" \
  "s_waitcnt lgkmcnt(6)\n" \
  "v_mfma_f32_16x16x32_bf16 v[48:51], v[12:15], v[32:35], v[48:51]\n" \
  "ds_read_b64_tr_b16 v[28:29], v4 offset:20512\n" \
  "v_cvt_pk_bf16_f32 v88, v88, v89\n" \
  "v_cvt_pk_bf16_f32 v89, v90, v91\n" \
  "v_mfma_f32_16x16x32_bf16 v[68:71], v[12:15], v[36:39], v[68:71]\n" \
  "ds_read_b64_tr_b16 v[30:31], v4 offset:21792\n" \
  "v_cvt_pk_bf16_f32 v90, v92, v93\n" \
  "s_waitcnt lgkmcnt(6)\n" \
  "v_mfma_f32_16x16x32_bf16 v[52:55], v[16:19], v[32:35], v[52:55]\n" \
  "ds_read_b64_tr_b16 v[12:13], v4 offset:25600\n" \
  "v_cvt_pk_bf16_f32 v91, v94, v95\n" \
  "v_cvt_pk_bf16_f32 v92, v96, v97\n" \
  "v_mfma_f32_16x16x32_bf16 v[72:75], v[16:19], v[36:39], v[72:75]\n" \
  "ds_read_b64_tr_b16 v[14:15], v4 offset:26880\n" \
  "ds_write_b64 v10, v[88:89] offset:10752\n" \
  "s_waitcnt lgkmcnt(7)\n" \
  "v_mfma_f32_16x16x32_bf16 v[56:59], v[20:23], v[32:35], v[56:59]\n" \
  "ds_read_b64_tr_b16 v[16:17], v4 offset:25608\n" \
  "v_cvt_pk_bf16_f32 v93, v98, v99\n" \
  "v_cvt_pk_bf16_f32 v94, v100, v101\n" \
  "v_mfma_f32_16x16x32_bf16 v[76:79], v[20:23], v[36:39], v[76:79]\n" \
  "ds_read_b64_tr_b16 v[18:19], v4 offset:26888\n" \
  "ds_write_b64 v10, v[90:91] offset:10760\n" \
  "s_waitcnt lgkmcnt(8)\n" \
  "v_mfma_f32_16x16x32_bf16 v[60:63], v[24:27], v[32:35], v[60:63]\n" \
  "ds_read_b64_tr_b16 v[20:21], v4 offset:25616\n" \
  "v_cvt_pk_bf16_f32 v95, v102, v103\n" \
  "v_cvt_pk_bf16_f32 v96, v104, v105\n" \
  "v_mfma_f32_16x16x32_bf16 v[80:83], v[24:27], v[36:39], v[80:83]\n" \
  "ds_read_b64_tr_b16 v[22:23], v4 offset:26896\n" \
  "ds_write_b64 v10, v[92:93] offset:10768\n" \
  "s_waitcnt lgkmcnt(9)\n" \
  "v_mfma_f32_16x16x32_bf16 v[64:67], v[28:31], v[32:35], v[64:67]\n" \
  "ds_read_b64_tr_b16 v[24:25], v4 offset:25624\n" \
  "v_cvt_pk_bf16_f32 v97, v106, v107\n" \
  "v_cvt_pk_bf16_f32 v98, v108, v109\n" \
  "v_mfma_f32_16x16x32_bf16 v[84:87], v[28:31], v[36:39], v[84:87]\n" \
  "ds_read_b64_tr_b16 v[26:27], v4 offset:26904\n" \
  "ds_write_b64 v10, v[94:95] offset:10776\n" \
  "v_cvt_pk_bf16_f32 v99, v110, v111\n" \
  "s_waitcnt lgkmcnt(10)\n" \
  "v_mfma_f32_16x16x32_bf16 v[48:51], v[12:15], v[40:43], v[48:51]\n" \
  "ds_read_b64_tr_b16 v[28:29], v4 offset:25632\n" \
  "v_cvt_pk_bf16_f32 v100, v112, v113\n" \
  "ds_write_b64 v10, v[96:97] offset:10784\n" \
  "v_mfma_f32_16x16x32_bf16 v[68:71], v[12:15], v[44:47], v[68:71]\n" \
  "ds_read_b64_tr_b16 v[30:31], v4 offset:26912\n" \
  "v_cvt_pk_bf16_f32 v101, v114, v115\n" \
  "v_cvt_pk_bf16_f32 v102, v116, v117\n" \
  "s_waitcnt lgkmcnt(10)\n" \
  "v_mfma_f32_16x16x32_bf16 v[52:55], v[16:19], v[40:43], v[52:55]\n" \
  "ds_write_b64 v10, v[98:99] offset:13440\n" \
  "v_cvt_pk_bf16_f32 v103, v118, v119\n" \
  "v_cvt_pk_bf16_f32 v104, v120, v121\n" \
  "v_mfma_f32_16x16x32_bf16 v[72:75], v[16:19], v[44:47], v[72:75]\n" \
  "ds_write_b64 v10, v[100:101] offset:13448\n" \
  "v_cvt_pk_bf16_f32 v105, v122, v123\n" \
  "v_cvt_pk_bf16_f32 v106, v124, v125\n" \
  "s_waitcnt lgkmcnt(9)\n" \
  "v_mfma_f32_16x16x32_bf16 v[56:59], v[20:23], v[40:43], v[56:59]\n" \
  "ds_write_b64 v10, v[102:103] offset:13456\n" \
  "v_cvt_pk_bf16_f32 v107, v126, v127\n" \
  "ds_write_b64 v10, v[104:105] offset:13464\n" \
  "v_mfma_f32_16x16x32_bf16 v[76:79], v[20:23], v[44:47], v[76:79]\n" \
  "ds_write_b64 v10, v[106:107] offset:13472\n" \
  "v_mov_b32_e32 v108, 0x3f80\n" \
  "v_mov_b32_e32 v109, 0\n" \
  "s_waitcnt lgkmcnt(9)\n" \
  "v_mfma_f32_16x16x32_bf16 v[60:63], v[24:27], v[40:43], v[60:63]\n" \
  "s_mov_b64 exec, 0xffffffff\n" \
  "ds_write_b64 v1, v[108:109] offset:10912\n" \
  "s_mov_b64 exec, -1\n" \
  "v_mfma_f32_16x16x32_bf16 v[80:83], v[24:27], v[44:47], v[80:83]\n" \
  "ds_add_u32 v3, v2 offset:16\n" \
  "ds_read_b64_tr_b16 v[88:89], v9 offset:0\n" \
  "ds_read_b64_tr_b16 v[90:91], v9 offset:160\n" \
  "s_waitcnt lgkmcnt(9)\n" \
  "v_mfma_f32_16x16x32_bf16 v[64:67], v[28:31], v[40:43], v[64:67]\n" \
  "ds_read_b64_tr_b16 v[92:93], v9 offset:320\n" \
  "ds_read_b64_tr_b16 v[94:95], v9 offset:480\n" \
  "ds_read_b64_tr_b16 v[96:97], v9 offset:640\n" \
  "v_mfma_f32_16x16x32_bf16 v[84:87], v[28:31], v[44:47], v[84:87]\n" \
  "ds_add_u32 v3, v2\n" \
  MID(0) \
  "s_cmp_lg_u32 s74, 0\n" \
  "s_cbranch_scc1 LFM\n" \
  "ds_read_b32 v11, v8 offset:40\n" \
  "ds_read_b128 v[96:99], v6 offset:0\n" \
  "ds_read_b128 v[100:103], v6 offset:2048\n" \
  "ds_read_b128 v[104:107], v7 offset:0\n" \
  "ds_read_b128 v[108:111], v7 offset:2048\n" \
  "ds_read_b128 v[112:115], v6 offset:4096\n" \
  "ds_read_b128 v[116:119], v6 offset:6144\n" \
  "ds_read_b128 v[120:123], v7 offset:4096\n" \
  "ds_read_b128 v[124:127], v7 offset:6144\n" \
  "v_cvt_pk_bf16_f32 v48, v48, v49\n" \
  "v_cvt_pk_bf16_f32 v49, v50, v51\n" \
  "v_cvt_pk_bf16_f32 v50, v52, v53\n" \
  "v_cvt_pk_bf16_f32 v51, v54, v55\n" \
  "v_cvt_pk_bf16_f32 v52, v56, v57\n" \
  "v_cvt_pk_bf16_f32 v53, v58, v59\n" \
  "v_cvt_pk_bf16_f32 v54, v60, v61\n" \
  "v_cvt_pk_bf16_f32 v55, v62, v63\n" \
  "v_cvt_pk_bf16_f32 v56, v64, v65\n" \
  "v_cvt_pk_bf16_f32 v57, v66, v67\n" \
  "v_cvt_pk_bf16_f32 v58, v68, v69\n" \
  "v_cvt_pk_bf16_f32 v59, v70, v71\n" \
  "v_cvt_pk_bf16_f32 v60, v72, v73\n" \
  "v_cvt_pk_bf16_f32 v61, v74, v75\n" \
  "v_cvt_pk_bf16_f32 v62, v76, v77\n" \
  "v_cvt_pk_bf16_f32 v63, v78, v79\n" \
  "v_cvt_pk_bf16_f32 v64, v80, v81\n" \
  "v_cvt_pk_bf16_f32 v65, v82, v83\n" \
  "v_cvt_pk_bf16_f32 v66, v84, v85\n" \
  "v_cvt_pk_bf16_f32 v67, v86, v87\n" \
  "s_waitcnt lgkmcnt(8)\n" \
  "v_cmp_gt_u32_e32 vcc, 1, v11\n" \
  "s_cbranch_vccnz LQa_s\n" \
  "LQa_k:\n" \
  "ds_read_b64_tr_b16 v[12:13], v4 offset:40960\n" \
  "ds_read_b64_tr_b16 v[14:15], v4 offset:42240\n" \
  "ds_read_b64_tr_b16 v[16:17], v4 offset:40968\n" \
  "ds_read_b64_tr_b16 v[18:19], v4 offset:42248\n" \
  "ds_read_b64_tr_b16 v[20:21], v4 offset:40976\n" \
  "ds_read_b64_tr_b16 v[22:23], v4 offset:42256\n" \
  "ds_read_b64_tr_b16 v[24:25], v4 offset:40984\n" \
  "ds_read_b64_tr_b16 v[26:27], v4 offset:42264\n" \
  "ds_write_b64 v10, v[48:49] offset:5376\n" \
  "ds_write_b64 v10, v[50:51] offset:5384\n" \
  "ds_write_b64 v10, v[52:53] offset:5392\n" \
  "ds_write_b64 v10, v[54:55] offset:5400\n" \
  "ds_write_b64 v10, v[56:57] offset:5408\n" \
  "ds_write_b64 v10, v[58:59] offset:8064\n" \
  "ds_write_b64 v10, v[60:61] offset:8072\n" \
  "ds_write_b64 v10, v[62:63] offset:8080\n" \
  "ds_write_b64 v10, v[64:65] offset:8088\n" \
  "ds_write_b64 v10, v[66:67] offset:8096\n" \
  "ds_add_u32 v3, v2 offset:16\n" \
  "s_branch LQS\n" \
  "LFM:\n" \
  FSx(24) \
  "ds_read_b64_tr_b16 v[22:23], v0 offset:0\n" \
  "ds_read_b64_tr_b16 v[24:25], v0 offset:160\n" \
  "ds_read_b64_tr_b16 v[32:33], v0 offset:800\n" \
  "ds_read_b64_tr_b16 v[34:35], v0 offset:960\n" \
  "ds_read_b64_tr_b16 v[26:27], v0 offset:320\n" \
  "ds_read_b64_tr_b16 v[28:29], v0 offset:480\n" \
  "ds_read_b64_tr_b16 v[36:37], v0 offset:1120\n" \
  "ds_read_b64_tr_b16 v[38:39], v0 offset:1280\n" \
  "ds_read_b64_tr_b16 v[30:31], v0 offset:640\n" \
  "ds_read_b64_tr_b16 v[40:41], v0 offset:1440\n" \
  "v_cvt_pk_bf16_f32 v108, v48, v49\n" \
  "v_cvt_pk_bf16_f32 v120, v68, v69\n" \
  "v_cvt_pk_bf16_f32 v109, v50, v51\n" \
  "v_cvt_pk_bf16_f32 v121, v70, v71\n" \
  "v_cvt_pk_bf16_f32 v110, v52, v53\n" \
  "v_cvt_pk_bf16_f32 v122, v72, v73\n" \
  "v_cvt_pk_bf16_f32 v111, v54, v55\n" \
  "v_cvt_pk_bf16_f32 v123, v74, v75\n" \
  "v_cvt_pk_bf16_f32 v112, v56, v57\n" \
  "v_cvt_pk_bf16_f32 v124, v76, v77\n" \
  "v_cvt_pk_bf16_f32 v113, v58, v59\n" \
  "v_cvt_pk_bf16_f32 v125, v78, v79\n" \
  "v_cvt_pk_bf16_f32 v114, v60, v61\n" \
  "v_cvt_pk_bf16_f32 v126, v80, v81\n" \
  "v_cvt_pk_bf16_f32 v115, v62, v63\n" \
  "v_cvt_pk_bf16_f32 v127, v82, v83\n" \
  "v_cvt_pk_bf16_f32 v118, v64, v65\n" \
  "v_cvt_pk_bf16_f32 v44, v84, v85\n" \
  "v_cvt_pk_bf16_f32 v119, v66, v67\n" \
  "v_cvt_pk_bf16_f32 v45, v86, v87\n" \
  "v_mov_b32_e32 v116, 0\n" \
  "v_mov_b32_e32 v117, 0\n" \
  "v_mov_b32_e32 v42, 0\n" \
  "v_mov_b32_e32 v43, 0\n" \
  FSx(26) \
  "s_waitcnt lgkmcnt(10)\n" \
  "v_mfma_f32_16x16x32_bf16 v[64:67], v[88:91], v[108:111], 0\n" \
  "v_mfma_f32_16x16x32_bf16 v[68:71], v[88:91], v[120:123], 0\n" \
  "v_mfma_f32_16x16x32_bf16 v[64:67], v[92:95], v[112:115], v[64:67]\n" \
  "v_permlane16_swap_b32_e32 v108, v120\n" \
  "v_permlane16_swap_b32_e32 v109, v121\n" \
  "v_mfma_f32_16x16x32_bf16 v[68:71], v[92:95], v[124:127], v[68:71]\n" \
  "v_permlane16_swap_b32_e32 v110, v122\n" \
  "v_permlane16_swap_b32_e32 v111, v123\n" \
  "v_mfma_f32_16x16x32_bf16 v[64:67], v[94:97], v[116:119], v[64:67]\n" \
  "v_permlane16_swap_b32_e32 v112, v124\n" \
  "v_permlane16_swap_b32_e32 v113, v125\n" \
  "v_mfma_f32_16x16x32_bf16 v[68:71], v[94:97], v[42:45], v[68:71]\n" \
  "v_permlane16_swap_b32_e32 v114, v126\n" \
  "v_permlane16_swap_b32_e32 v115, v127\n" \
  FSx(28) \
  "s_waitcnt lgkmcnt(0)\n" \
  "v_mfma_f32_32x32x16_bf16 v[48:63], v[22:25], v[108:111], 0\n" \
  "v_permlane16_swap_b32_e32 v118, v44\n" \
  "v_permlane16_swap_b32_e32 v119, v45\n" \
  "v_mfma_f32_32x32x16_bf16 v[72:87], v[32:35], v[120:123], 0\n" \
  "s_nop 3\n" \
  "v_permlane16_swap_b32_e32 v64, v68\n" \
  "v_permlane16_swap_b32_e32 v65, v69\n" \
  "v_mfma_f32_32x32x16_bf16 v[48:63], v[26:29], v[112:115], v[48:63]\n" \
  "v_permlane16_swap_b32_e32 v66, v70\n" \
  "v_permlane16_swap_b32_e32 v67, v71\n" \
  "v_mfma_f32_32x32x16_bf16 v[72:87], v[36:39], v[124:127], v[72:87]\n" \
  "ds_read_b128 v[96:99], v6 offset:0\n" \
  "ds_read_b128 v[100:103], v6 offset:2048\n" \
  "v_mfma_f32_32x32x16_bf16 v[48:63], v[28:31], v[116:119], v[48:63]\n" \
  "ds_read_b128 v[104:107], v7 offset:0\n" \
  "ds_read_b128 v[108:111], v7 offset:2048\n" \
  "v_mfma_f32_32x32x16_bf16 v[72:87], v[38:41], v[42:45], v[72:87]\n" \
  "s_nop 5\n" \
  FSx(30) \
  "v_max_f32_e32 v40, v64, v65\n" \
  "v_min_f32_e32 v41, v64, v65\n" \
  "v_max_f32_e32 v42, v66, v67\n" \
  "v_min_f32_e32 v43, v66, v67\n" \
  "ds_read_b128 v[112:115], v6 offset:4096\n" \
  "v_max3_f32 v40, v40, v48, v49\n" \
  "v_min3_f32 v41, v41, v48, v49\n" \
  "v_max3_f32 v42, v42, v50, v51\n" \
  "v_min3_f32 v43, v43, v50, v51\n" \
  "ds_read_b128 v[116:119], v6 offset:6144\n" \
  "v_max3_f32 v40, v40, v52, v53\n" \
  "v_min3_f32 v41, v41, v52, v53\n" \
  "v_max3_f32 v42, v42, v54, v55\n" \
  "v_min3_f32 v43, v43, v54, v55\n" \
  "ds_read_b128 v[120:123], v7 offset:4096\n" \
  "v_max3_f32 v40, v40, v56, v57\n" \
  "v_min3_f32 v41, v41, v56, v57\n" \
  "v_max3_f32 v42, v42, v58, v59\n" \
  "v_min3_f32 v43, v43, v58, v59\n" \
  "ds_read_b128 v[124:127], v7 offset:6144\n" \
  "v_max3_f32 v40, v40, v60, v61\n" \
  "v_min3_f32 v41, v41, v60, v61\n" \
  "v_max3_f32 v42, v42, v62, v63\n" \
  "v_min3_f32 v43, v43, v62, v63\n" \
  "v_max_f32_e32 v44, v40, v42\n" \
  "v_min_f32_e32 v46, v41, v43\n" \
  "v_mul_f32_e32 v44, s70, v44\n" \
  "v_mul_f32_e32 v46, s70, v46\n" \
  "v_pk_fma_f32 v[20:21], v[48:49], s[70:71], v[46:47] op_sel_hi:[1,0,0] neg_lo:[1,0,0] neg_hi:[1,0,0]\n" \
  "v_pk_fma_f32 v[48:49], v[48:49], s[70:71], v[44:45] op_sel_hi:[1,0,0] neg_lo:[0,0,1] neg_hi:[0,0,1]\n" \
  "v_pk_fma_f32 v[22:23], v[50:51], s[70:71], v[46:47] op_sel_hi:[1,0,0] neg_lo:[1,0,0] neg_hi:[1,0,0]\n" \
  "v_pk_fma_f32 v[50:51], v[50:51], s[70:71], v[44:45] op_sel_hi:[1,0,0] neg_lo:[0,0,1] neg_hi:[0,0,1]\n" \
  "v_pk_fma_f32 v[24:25], v[52:53], s[70:71], v[46:47] op_sel_hi:[1,0,0] neg_lo:[1,0,0] neg_hi:[1,0,0]\n" \
  "v_pk_fma_f32 v[52:53], v[52:53], s[70:71], v[44:45] op_sel_hi:[1,0,0] neg_lo:[0,0,1] neg_hi:[0,0,1]\n" \
  "v_pk_fma_f32 v[26:27], v[54:55], s[70:71], v[46:47] op_sel_hi:[1,0,0] neg_lo:[1,0,0] neg_hi:[1,0,0]\n" \
  "v_pk_fma_f32 v[54:55], v[54:55], s[70:71], v[44:45] op_sel_hi:[1,0,0] neg_lo:[0,0,1] neg_hi:[0,0,1]\n" \
  "v_pk_fma_f32 v[28:29], v[56:57], s[70:71], v[46:47] op_sel_hi:[1,0,0] neg_lo:[1,0,0] neg_hi:[1,0,0]\n" \
  "v_pk_fma_f32 v[56:57], v[56:57], s[70:71], v[44:45] op_sel_hi:[1,0,0] neg_lo:[0,0,1] neg_hi:[0,0,1]\n" \
  "v_pk_fma_f32 v[30:31], v[58:59], s[70:71], v[46:47] op_sel_hi:[1,0,0] neg_lo:[1,0,0] neg_hi:[1,0,0]\n" \
  "v_pk_fma_f32 v[58:59], v[58:59], s[70:71], v[44:45] op_sel_hi:[1,0,0] neg_lo:[0,0,1] neg_hi:[0,0,1]\n" \
  "v_pk_fma_f32 v[32:33], v[60:61], s[70:71], v[46:47] op_sel_hi:[1,0,0] neg_lo:[1,0,0] neg_hi:[1,0,0]\n" \
  "v_pk_fma_f32 v[60:61], v[60:61], s[70:71], v[44:45] op_sel_hi:[1,0,0] neg_lo:[0,0,1] neg_hi:[0,0,1]\n" \
  "v_pk_fma_f32 v[34:35], v[62:63], s[70:71], v[46:47] op_sel_hi:[1,0,0] neg_lo:[1,0,0] neg_hi:[1,0,0]\n" \
  "v_pk_fma_f32 v[62:63], v[62:63], s[70:71], v[44:45] op_sel_hi:[1,0,0] neg_lo:[0,0,1] neg_hi:[0,0,1]\n" \
  "v_pk_fma_f32 v[36:37], v[64:65], s[70:71], v[46:47] op_sel_hi:[1,0,0] neg_lo:[1,0,0] neg_hi:[1,0,0]\n" \
  "v_pk_fma_f32 v[64:65], v[64:65], s[70:71], v[44:45] op_sel_hi:[1,0,0] neg_lo:[0,0,1] neg_hi:[0,0,1]\n" \
  "v_pk_fma_f32 v[38:39], v[66:67], s[70:71], v[46:47] op_sel_hi:[1,0,0] neg_lo:[1,0,0] neg_hi:[1,0,0]\n" \
  "v_pk_fma_f32 v[66:67], v[66:67], s[70:71], v[44:45] op_sel_hi:[1,0,0] neg_lo:[0,0,1] neg_hi:[0,0,1]\n" \
  FSx(32) \
  "v_exp_f32_e32 v48, v48\n" \
  "v_exp_f32_e32 v20, v20\n" \
  "v_exp_f32_e32 v49, v49\n" \
  "v_exp_f32_e32 v21, v21\n" \
  "v_exp_f32_e32 v50, v50\n" \
  "v_exp_f32_e32 v22, v22\n" \
  "v_exp_f32_e32 v51, v51\n" \
  "v_exp_f32_e32 v23, v23\n" \
  "v_exp_f32_e32 v52, v52\n" \
  "v_exp_f32_e32 v24, v24\n" \
  "v_exp_f32_e32 v53, v53\n" \
  "v_exp_f32_e32 v25, v25\n" \
  "v_exp_f32_e32 v54, v54\n" \
  "v_exp_f32_e32 v26, v26\n" \
  "v_exp_f32_e32 v55, v55\n" \
  "v_exp_f32_e32 v27, v27\n" \
  "v_exp_f32_e32 v56, v56\n" \
  "v_exp_f32_e32 v28, v28\n" \
  "v_exp_f32_e32 v57, v57\n" \
  "v_exp_f32_e32 v29, v29\n" \
  "v_exp_f32_e32 v58, v58\n" \
  "v_exp_f32_e32 v30, v30\n" \
  "v_exp_f32_e32 v59, v59\n" \
  "v_exp_f32_e32 v31, v31\n" \
  "v_exp_f32_e32 v60, v60\n" \
  "v_exp_f32_e32 v32, v32\n" \
  "v_exp_f32_e32 v61, v61\n" \
  "v_exp_f32_e32 v33, v33\n" \
  "v_exp_f32_e32 v62, v62\n" \
  "v_exp_f32_e32 v34, v34\n" \
  "v_exp_f32_e32 v63, v63\n" \
  "v_exp_f32_e32 v35, v35\n" \
  "v_exp_f32_e32 v64, v64\n" \
  "v_exp_f32_e32 v36, v36\n" \
  "v_exp_f32_e32 v65, v65\n" \
  "v_exp_f32_e32 v37, v37\n" \
  "v_exp_f32_e32 v66, v66\n" \
  "v_exp_f32_e32 v38, v38\n" \
  "v_exp_f32_e32 v67, v67\n" \
  "v_exp_f32_e32 v39, v39\n" \
  FSx(34) \
  "v_pk_add_f32 v[44:45], v[48:49], v[50:51]\n" \
  "v_pk_add_f32 v[46:47], v[20:21], v[22:23]\n" \
  "v_pk_add_f32 v[44:45], v[44:45], v[52:53]\n" \
  "v_pk_add_f32 v[46:47], v[46:47], v[24:25]\n" \
  "v_pk_add_f32 v[44:45], v[44:45], v[54:55]\n" \
  "v_pk_add_f32 v[46:47], v[46:47], v[26:27]\n" \
  "v_pk_add_f32 v[44:45], v[44:45], v[56:57]\n" \
  "v_pk_add_f32 v[46:47], v[46:47], v[28:29]\n" \
  "v_pk_add_f32 v[44:45], v[44:45], v[58:59]\n" \
  "v_pk_add_f32 v[46:47], v[46:47], v[30:31]\n" \
  "v_pk_add_f32 v[44:45], v[44:45], v[60:61]\n" \
  "v_pk_add_f32 v[46:47], v[46:47], v[32:33]\n" \
  "v_pk_add_f32 v[44:45], v[44:45], v[62:63]\n" \
  "v_pk_add_f32 v[46:47], v[46:47], v[34:35]\n" \
  "v_pk_add_f32 v[44:45], v[44:45], v[64:65]\n" \
  "v_pk_add_f32 v[46:47], v[46:47], v[36:37]\n" \
  "v_pk_add_f32 v[44:45], v[44:45], v[66:67]\n" \
  "v_pk_add_f32 v[46:47], v[46:47], v[38:39]\n" \
  "v_add_f32_e32 v44, v44, v45\n" \
  "v_add_f32_e32 v46, v46, v47\n" \
  "v_rcp_f32_e32 v44, v44\n" \
  "v_rcp_f32_e32 v46, v46\n" \
  FSx(36) \
  "v_pk_mul_f32 v[48:49], v[48:49], v[44:45] op_sel_hi:[1,0]\n" \
  "v_pk_mul_f32 v[50:51], v[50:51], v[44:45] op_sel_hi:[1,0]\n" \
  "v_pk_mul_f32 v[52:53], v[52:53], v[44:45] op_sel_hi:[1,0]\n" \
  "v_pk_mul_f32 v[54:55], v[54:55], v[44:45] op_sel_hi:[1,0]\n" \
  "v_cvt_pk_bf16_f32 v10, v48, v49\n" \
  "v_cvt_pk_bf16_f32 v11, v50, v51\n" \
  "ds_write_b64 v1, v[10:11] offset:0\n" \
  "v_pk_mul_f32 v[56:57], v[56:57], v[44:45] op_sel_hi:[1,0]\n" \
  "v_pk_mul_f32 v[58:59], v[58:59], v[44:45] op_sel_hi:[1,0]\n" \
  "v_cvt_pk_bf16_f32 v12, v52, v53\n" \
  "v_cvt_pk_bf16_f32 v13, v54, v55\n" \
  "ds_write_b64 v1, v[12:13] offset:8\n" \
  "v_pk_mul_f32 v[60:61], v[60:61], v[44:45] op_sel_hi:[1,0]\n" \
  "v_pk_mul_f32 v[62:63], v[62:63], v[44:45] op_sel_hi:[1,0]\n" \
  "v_cvt_pk_bf16_f32 v14, v56, v57\n" \
  "v_cvt_pk_bf16_f32 v15, v58, v59\n" \
  "ds_write_b64 v1, v[14:15] offset:16\n" \
  "v_pk_mul_f32 v[64:65], v[64:65], v[44:45] op_sel_hi:[1,0]\n" \
  "v_pk_mul_f32 v[66:67], v[66:67], v[44:45] op_sel_hi:[1,0]\n" \
  "v_cvt_pk_bf16_f32 v16, v60, v61\n" \
  "v_cvt_pk_bf16_f32 v17, v62, v63\n" \
  "ds_write_b64 v1, v[16:17] offset:24\n" \
  "v_pk_mul_f32 v[20:21], v[20:21], v[46:47] op_sel_hi:[1,0]\n" \
  "v_pk_mul_f32 v[22:23], v[22:23], v[46:47] op_sel_hi:[1,0]\n" \
  "v_cvt_pk_bf16_f32 v18, v64, v65\n" \
  "v_cvt_pk_bf16_f32 v19, v66, v67\n" \
  "ds_write_b64 v1, v[18:19] offset:32\n" \
  "v_pk_mul_f32 v[24:25], v[24:25], v[46:47] op_sel_hi:[1,0]\n" \
  "v_pk_mul_f32 v[26:27], v[26:27], v[46:47] op_sel_hi:[1,0]\n" \
  "v_cvt_pk_bf16_f32 v20, v20, v21\n" \
  "v_cvt_pk_bf16_f32 v21, v22, v23\n" \
  "v_pk_mul_f32 v[28:29], v[28:29], v[46:47] op_sel_hi:[1,0]\n" \
  "v_pk_mul_f32 v[30:31], v[30:31], v[46:47] op_sel_hi:[1,0]\n" \
  "v_cvt_pk_bf16_f32 v22, v24, v25\n" \
  "v_cvt_pk_bf16_f32 v23, v26, v27\n" \
  "v_pk_mul_f32 v[32:33], v[32:33], v[46:47] op_sel_hi:[1,0]\n" \
  "v_pk_mul_f32 v[34:35], v[34:35], v[46:47] op_sel_hi:[1,0]\n" \
  "v_cvt_pk_bf16_f32 v24, v28, v29\n" \
  "v_cvt_pk_bf16_f32 v25, v30, v31\n" \
  "v_pk_mul_f32 v[36:37], v[36:37], v[46:47] op_sel_hi:[1,0]\n" \
  "v_pk_mul_f32 v[38:39], v[38:39], v[46:47] op_sel_hi:[1,0]\n" \
  "v_cvt_pk_bf16_f32 v26, v32, v33\n" \
  "v_cvt_pk_bf16_f32 v27, v34, v35\n" \
  "v_cvt_pk_bf16_f32 v28, v36, v37\n" \
  "v_cvt_pk_bf16_f32 v29, v38, v39\n" \
  FSx(38) \
  FSx(40) \
  "v_max_f32_e32 v60, v68, v69\n" \
  "v_min_f32_e32 v61, v68, v69\n" \
  "v_max_f32_e32 v62, v70, v71\n" \
  "v_min_f32_e32 v63, v70, v71\n" \
  "ds_write_b64 v1, v[20:21] offset:40\n" \
  "v_max3_f32 v60, v60, v72, v73\n" \
  "v_min3_f32 v61, v61, v72, v73\n" \
  "v_max3_f32 v62, v62, v74, v75\n" \
  "v_min3_f32 v63, v63, v74, v75\n" \
  "v_max3_f32 v60, v60, v76, v77\n" \
  "v_min3_f32 v61, v61, v76, v77\n" \
  "ds_write_b64 v1, v[22:23] offset:48\n" \
  "v_max3_f32 v62, v62, v78, v79\n" \
  "v_min3_f32 v63, v63, v78, v79\n" \
  "v_max3_f32 v60, v60, v80, v81\n" \
  "v_min3_f32 v61, v61, v80, v81\n" \
  "v_max3_f32 v62, v62, v82, v83\n" \
  "v_min3_f32 v63, v63, v82, v83\n" \
  "ds_write_b64 v1, v[24:25] offset:56\n" \
  "v_max3_f32 v60, v60, v84, v85\n" \
  "v_min3_f32 v61, v61, v84, v85\n" \
  "v_max3_f32 v62, v62, v86, v87\n" \
  "v_min3_f32 v63, v63, v86, v87\n" \
  "v_max_f32_e32 v64, v60, v62\n" \
  "v_min_f32_e32 v66, v61, v63\n" \
  "ds_write_b64 v1, v[26:27] offset:64\n" \
  "v_mul_f32_e32 v64, s70, v64\n" \
  "v_mul_f32_e32 v66, s70, v66\n" \
  "v_pk_fma_f32 v[40:41], v[72:73], s[70:71], v[66:67] op_sel_hi:[1,0,0] neg_lo:[1,0,0] neg_hi:[1,0,0]\n" \
  "v_pk_fma_f32 v[72:73], v[72:73], s[70:71], v[64:65] op_sel_hi:[1,0,0] neg_lo:[0,0,1] neg_hi:[0,0,1]\n" \
  "v_pk_fma_f32 v[42:43], v[74:75], s[70:71], v[66:67] op_sel_hi:[1,0,0] neg_lo:[1,0,0] neg_hi:[1,0,0]\n" \
  "v_pk_fma_f32 v[74:75], v[74:75], s[70:71], v[64:65] op_sel_hi:[1,0,0] neg_lo:[0,0,1] neg_hi:[0,0,1]\n" \
  "ds_write_b64 v1, v[28:29] offset:72\n" \
  "v_pk_fma_f32 v[44:45], v[76:77], s[70:71], v[66:67] op_sel_hi:[1,0,0] neg_lo:[1,0,0] neg_hi:[1,0,0]\n" \
  "v_pk_fma_f32 v[76:77], v[76:77], s[70:71], v[64:65] op_sel_hi:[1,0,0] neg_lo:[0,0,1] neg_hi:[0,0,1]\n" \
  "v_pk_fma_f32 v[46:47], v[78:79], s[70:71], v[66:67] op_sel_hi:[1,0,0] neg_lo:[1,0,0] neg_hi:[1,0,0]\n" \
  "v_pk_fma_f32 v[78:79], v[78:79], s[70:71], v[64:65] op_sel_hi:[1,0,0] neg_lo:[0,0,1] neg_hi:[0,0,1]\n" \
  "v_pk_fma_f32 v[48:49], v[80:81], s[70:71], v[66:67] op_sel_hi:[1,0,0] neg_lo:[1,0,0] neg_hi:[1,0,0]\n" \
  "v_pk_fma_f32 v[80:81], v[80:81], s[70:71], v[64:65] op_sel_hi:[1,0,0] neg_lo:[0,0,1] neg_hi:[0,0,1]\n" \
  "ds_add_u32 v3, v2 offset:16\n" \
  "v_pk_fma_f32 v[50:51], v[82:83], s[70:71], v[66:67] op_sel_hi:[1,0,0] neg_lo:[1,0,0] neg_hi:[1,0,0]\n" \
  "v_pk_fma_f32 v[82:83], v[82:83], s[70:71], v[64:65] op_sel_hi:[1,0,0] neg_lo:[0,0,1] neg_hi:[0,0,1]\n" \
  "v_pk_fma_f32 v[52:53], v[84:85], s[70:71], v[66:67] op_sel_hi:[1,0,0] neg_lo:[1,0,0] neg_hi:[1,0,0]\n" \
  "v_pk_fma_f32 v[84:85], v[84:85], s[70:71], v[64:65] op_sel_hi:[1,0,0] neg_lo:[0,0,1] neg_hi:[0,0,1]\n" \
  "v_pk_fma_f32 v[54:55], v[86:87], s[70:71], v[66:67] op_sel_hi:[1,0,0] neg_lo:[1,0,0] neg_hi:[1,0,0]\n" \
  "v_pk_fma_f32 v[86:87], v[86:87], s[70:71], v[64:65] op_sel_hi:[1,0,0] neg_lo:[0,0,1] neg_hi:[0,0,1]\n" \
  "v_pk_fma_f32 v[56:57], v[68:69], s[70:71], v[66:67] op_sel_hi:[1,0,0] neg_lo:[1,0,0] neg_hi:[1,0,0]\n" \
  "v_pk_fma_f32 v[68:69], v[68:69], s[70:71], v[64:65] op_sel_hi:[1,0,0] neg_lo:[0,0,1] neg_hi:[0,0,1]\n" \
  "v_pk_fma_f32 v[58:59], v[70:71], s[70:71], v[66:67] op_sel_hi:[1,0,0] neg_lo:[1,0,0] neg_hi:[1,0,0]\n" \
  "v_pk_fma_f32 v[70:71], v[70:71], s[70:71], v[64:65] op_sel_hi:[1,0,0] neg_lo:[0,0,1] neg_hi:[0,0,1]\n" \
  FSx(42) \
  "v_exp_f32_e32 v72, v72\n" \
  "v_exp_f32_e32 v40, v40\n" \
  "v_exp_f32_e32 v73, v73\n" \
  "v_exp_f32_e32 v41, v41\n" \
  "v_exp_f32_e32 v74, v74\n" \
  "v_exp_f32_e32 v42, v42\n" \
  "v_exp_f32_e32 v75, v75\n" \
  "v_exp_f32_e32 v43, v43\n" \
  "v_exp_f32_e32 v76, v76\n" \
  "v_exp_f32_e32 v44, v44\n" \
  "v_exp_f32_e32 v77, v77\n" \
  "v_exp_f32_e32 v45, v45\n" \
  "v_exp_f32_e32 v78, v78\n" \
  "v_exp_f32_e32 v46, v46\n" \
  "v_exp_f32_e32 v79, v79\n" \
  "v_exp_f32_e32 v47, v47\n" \
  "v_exp_f32_e32 v80, v80\n" \
  "v_exp_f32_e32 v48, v48\n" \
  "v_exp_f32_e32 v81, v81\n" \
  "v_exp_f32_e32 v49, v49\n" \
  "ds_read_b32 v11, v8 offset:40\n" \
  "v_exp_f32_e32 v82, v82\n" \
  "v_exp_f32_e32 v50, v50\n" \
  "v_exp_f32_e32 v83, v83\n" \
  "v_exp_f32_e32 v51, v51\n" \
  "v_exp_f32_e32 v84, v84\n" \
  "v_exp_f32_e32 v52, v52\n" \
  "v_exp_f32_e32 v85, v85\n" \
  "v_exp_f32_e32 v53, v53\n" \
  "v_exp_f32_e32 v86, v86\n" \
  "v_exp_f32_e32 v54, v54\n" \
  "v_exp_f32_e32 v87, v87\n" \
  "v_exp_f32_e32 v55, v55\n" \
  "v_exp_f32_e32 v68, v68\n" \
  "v_exp_f32_e32 v56, v56\n" \
  "v_exp_f32_e32 v69, v69\n" \
  "v_exp_f32_e32 v57, v57\n" \
  "v_exp_f32_e32 v70, v70\n" \
  "v_exp_f32_e32 v58, v58\n" \
  "v_exp_f32_e32 v71, v71\n" \
  "v_exp_f32_e32 v59, v59\n" \
  FSx(44) \
  "s_waitcnt lgkmcnt(0)\n" \
  "v_cmp_gt_u32_e32 vcc, 1, v11\n" \
  "s_cbranch_vccnz LQb_s\n" \
  "LQb_k:\n" \
  "ds_read_b64_tr_b16 v[12:13], v4 offset:40960\n" \
  "ds_read_b64_tr_b16 v[14:15], v4 offset:42240\n" \
  "ds_read_b64_tr_b16 v[16:17], v4 offset:40968\n" \
  "ds_read_b64_tr_b16 v[18:19], v4 offset:42248\n" \
  "ds_read_b64_tr_b16 v[20:21], v4 offset:40976\n" \
  "ds_read_b64_tr_b16 v[22:23], v4 offset:42256\n" \
  "ds_read_b64_tr_b16 v[24:25], v4 offset:40984\n" \
  "ds_read_b64_tr_b16 v[26:27], v4 offset:42264\n" \
  "v_pk_add_f32 v[64:65], v[72:73], v[74:75]\n" \
  "v_pk_add_f32 v[66:67], v[40:41], v[42:43]\n" \
  "v_pk_add_f32 v[64:65], v[64:65], v[76:77]\n" \
  "v_pk_add_f32 v[66:67], v[66:67], v[44:45]\n" \
  "v_pk_add_f32 v[64:65], v[64:65], v[78:79]\n" \
  "v_pk_add_f32 v[66:67], v[66:67], v[46:47]\n" \
  "v_pk_add_f32 v[64:65], v[64:65], v[80:81]\n" \
  "v_pk_add_f32 v[66:67], v[66:67], v[48:49]\n" \
  "v_pk_add_f32 v[64:65], v[64:65], v[82:83]\n" \
  "v_pk_add_f32 v[66:67], v[66:67], v[50:51]\n" \
  "v_pk_add_f32 v[64:65], v[64:65], v[84:85]\n" \
  "v_pk_add_f32 v[66:67], v[66:67], v[52:53]\n" \
  "v_pk_add_f32 v[64:65], v[64:65], v[86:87]\n" \
  "v_pk_add_f32 v[66:67], v[66:67], v[54:55]\n" \
  "v_pk_add_f32 v[64:65], v[64:65], v[68:69]\n" \
  "v_pk_add_f32 v[66:67], v[66:67], v[56:57]\n" \
  "v_pk_add_f32 v[64:65], v[64:65], v[70:71]\n" \
  "v_pk_add_f32 v[66:67], v[66:67], v[58:59]\n" \
  "v_add_f32_e32 v64, v64, v65\n" \
  "v_add_f32_e32 v66, v66, v67\n" \
  "v_rcp_f32_e32 v64, v64\n" \
  "v_rcp_f32_e32 v66, v66\n" \
  FSx(46) \
  "v_pk_mul_f32 v[72:73], v[72:73], v[64:65] op_sel_hi:[1,0]\n" \
  "v_pk_mul_f32 v[74:75], v[74:75], v[64:65] op_sel_hi:[1,0]\n" \
  "v_pk_mul_f32 v[76:77], v[76:77], v[64:65] op_sel_hi:[1,0]\n" \
  "v_pk_mul_f32 v[78:79], v[78:79], v[64:65] op_sel_hi:[1,0]\n" \
  "v_cvt_pk_bf16_f32 v30, v72, v73\n" \
  "v_cvt_pk_bf16_f32 v31, v74, v75\n" \
  "ds_write_b64 v1, v[30:31] offset:80\n" \
  "v_pk_mul_f32 v[80:81], v[80:81], v[64:65] op_sel_hi:[1,0]\n" \
  "v_pk_mul_f32 v[82:83], v[82:83], v[64:65] op_sel_hi:[1,0]\n" \
  "v_cvt_pk_bf16_f32 v32, v76, v77\n" \
  "v_cvt_pk_bf16_f32 v33, v78, v79\n" \
  "ds_write_b64 v1, v[32:33] offset:88\n" \
  "v_pk_mul_f32 v[84:85], v[84:85], v[64:65] op_sel_hi:[1,0]\n" \
  "v_pk_mul_f32 v[86:87], v[86:87], v[64:65] op_sel_hi:[1,0]\n" \
  "v_cvt_pk_bf16_f32 v34, v80, v81\n" \
  "v_cvt_pk_bf16_f32 v35, v82, v83\n" \
  "ds_write_b64 v1, v[34:35] offset:96\n" \
  "v_pk_mul_f32 v[68:69], v[68:69], v[64:65] op_sel_hi:[1,0]\n" \
  "v_pk_mul_f32 v[70:71], v[70:71], v[64:65] op_sel_hi:[1,0]\n" \
  "v_cvt_pk_bf16_f32 v36, v84, v85\n" \
  "v_cvt_pk_bf16_f32 v37, v86, v87\n" \
  "ds_write_b64 v1, v[36:37] offset:104\n" \
  "v_pk_mul_f32 v[40:41], v[40:41], v[66:67] op_sel_hi:[1,0]\n" \
  "v_pk_mul_f32 v[42:43], v[42:43], v[66:67] op_sel_hi:[1,0]\n" \
  "v_cvt_pk_bf16_f32 v38, v68, v69\n" \
  "v_cvt_pk_bf16_f32 v39, v70, v71\n" \
  "ds_write_b64 v1, v[38:39] offset:112\n" \
  "v_pk_mul_f32 v[44:45], v[44:45], v[66:67] op_sel_hi:[1,0]\n" \
  "v_pk_mul_f32 v[46:47], v[46:47], v[66:67] op_sel_hi:[1,0]\n" \
  "v_cvt_pk_bf16_f32 v40, v40, v41\n" \
  "v_cvt_pk_bf16_f32 v41, v42, v43\n" \
  "ds_write_b64 v1, v[40:41] offset:120\n" \
  "v_pk_mul_f32 v[48:49], v[48:49], v[66:67] op_sel_hi:[1,0]\n" \
  "v_pk_mul_f32 v[50:51], v[50:51], v[66:67] op_sel_hi:[1,0]\n" \
  "v_cvt_pk_bf16_f32 v42, v44, v45\n" \
  "v_cvt_pk_bf16_f32 v43, v46, v47\n" \
  "ds_write_b64 v1, v[42:43] offset:128\n" \
  "v_pk_mul_f32 v[52:53], v[52:53], v[66:67] op_sel_hi:[1,0]\n" \
  "v_pk_mul_f32 v[54:55], v[54:55], v[66:67] op_sel_hi:[1,0]\n" \
  "v_cvt_pk_bf16_f32 v44, v48, v49\n" \
  "v_cvt_pk_bf16_f32 v45, v50, v51\n" \
  "ds_write_b64 v1, v[44:45] offset:136\n" \
  "v_pk_mul_f32 v[56:57], v[56:57], v[66:67] op_sel_hi:[1,0]\n" \
  "v_pk_mul_f32 v[58:59], v[58:59], v[66:67] op_sel_hi:[1,0]\n" \
  "v_cvt_pk_bf16_f32 v46, v52, v53\n" \
  "v_cvt_pk_bf16_f32 v47, v54, v55\n" \
  "ds_write_b64 v1, v[46:47] offset:144\n" \
  "v_cvt_pk_bf16_f32 v48, v56, v57\n" \
  "v_cvt_pk_bf16_f32 v49, v58, v59\n" \
  "ds_write_b64 v1, v[48:49] offset:152\n" \
  "ds_add_u32 v3, v2 offset:16\n" \
  FSx(48) \
  "LQS:\n" \
  MID(1) \
  "s_waitcnt lgkmcnt(11)\n" \
  "v_mfma_f32_16x16x32_bf16 v[48:51], v[12:15], a[0:3], 0\n" \
  "ds_read_b64_tr_b16 v[28:29], v4 offset:40992\n" \
  "v_mfma_f32_16x16x32_bf16 v[68:71], v[12:15], a[4:7], 0\n" \
  "ds_read_b64_tr_b16 v[30:31], v4 offset:42272\n" \
  "v_mfma_f32_16x16x32_bf16 v[52:55], v[16:19], a[0:3], 0\n" \
  "ds_read_b64_tr_b16 v[12:13], v4 offset:46080\n" \
  "v_mfma_f32_16x16x32_bf16 v[72:75], v[16:19], a[4:7], 0\n" \
  "ds_read_b64_tr_b16 v[14:15], v4 offset:47360\n" \
  "v_mfma_f32_16x16x32_bf16 v[56:59], v[20:23], a[0:3], 0\n" \
  "ds_read_b64_tr_b16 v[16:17], v4 offset:46088\n" \
  "v_mfma_f32_16x16x32_bf16 v[76:79], v[20:23], a[4:7], 0\n" \
  "ds_read_b64_tr_b16 v[18:19], v4 offset:47368\n" \
  "v_mfma_f32_16x16x32_bf16 v[60:63], v[24:27], a[0:3], 0\n" \
  "ds_read_b64_tr_b16 v[20:21], v4 offset:46096\n" \
  "v_mfma_f32_16x16x32_bf16 v[80:83], v[24:27], a[4:7], 0\n" \
  "ds_read_b64_tr_b16 v[22:23], v4 offset:47376\n" \
  "s_waitcnt lgkmcnt(6)\n" \
  "v_mfma_f32_16x16x32_bf16 v[64:67], v[28:31], a[0:3], 0\n" \
  "ds_read_b64_tr_b16 v[24:25], v4 offset:46104\n" \
  "v_mfma_f32_16x16x32_bf16 v[84:87], v[28:31], a[4:7], 0\n" \
  "ds_read_b64_tr_b16 v[26:27], v4 offset:47384\n" \
  "s_waitcnt lgkmcnt(6)\n" \
  "v_mfma_f32_16x16x32_bf16 v[48:51], v[12:15], a[8:11], v[48:51]\n" \
  "ds_read_b64_tr_b16 v[28:29], v4 offset:46112\n" \
  "v_mfma_f32_16x16x32_bf16 v[68:71], v[12:15], a[12:15], v[68:71]\n" \
  "ds_read_b64_tr_b16 v[30:31], v4 offset:47392\n" \
  "s_waitcnt lgkmcnt(6)\n" \
  "v_mfma_f32_16x16x32_bf16 v[52:55], v[16:19], a[8:11], v[52:55]\n" \
  "ds_read_b64_tr_b16 v[12:13], v4 offset:51200\n" \
  "v_mfma_f32_16x16x32_bf16 v[72:75], v[16:19], a[12:15], v[72:75]\n" \
  "ds_read_b64_tr_b16 v[14:15], v4 offset:52480\n" \
  "s_waitcnt lgkmcnt(6)\n" \
  "v_mfma_f32_16x16x32_bf16 v[56:59], v[20:23], a[8:11], v[56:59]\n" \
  "ds_read_b64_tr_b16 v[16:17], v4 offset:51208\n" \
  "v_mfma_f32_16x16x32_bf16 v[76:79], v[20:23], a[12:15], v[76:79]\n" \
  "ds_read_b64_tr_b16 v[18:19], v4 offset:52488\n" \
  "s_waitcnt lgkmcnt(6)\n" \
  "v_mfma_f32_16x16x32_bf16 v[60:63], v[24:27], a[8:11], v[60:63]\n" \
  "ds_read_b64_tr_b16 v[20:21], v4 offset:51216\n" \
  "ds_read_b32 v11, v8 offset:44\n" \
  "v_mfma_f32_16x16x32_bf16 v[80:83], v[24:27], a[12:15], v[80:83]\n" \
  "ds_read_b64_tr_b16 v[22:23], v4 offset:52496\n" \
  "s_waitcnt lgkmcnt(7)\n" \
  "v_mfma_f32_16x16x32_bf16 v[64:67], v[28:31], a[8:11], v[64:67]\n" \
  "ds_read_b64_tr_b16 v[24:25], v4 offset:51224\n" \
  "v_mfma_f32_16x16x32_bf16 v[84:87], v[28:31], a[12:15], v[84:87]\n" \
  "ds_read_b64_tr_b16 v[26:27], v4 offset:52504\n" \
  "s_waitcnt lgkmcnt(7)\n" \
  "v_mfma_f32_16x16x32_bf16 v[48:51], v[12:15], a[16:19], v[48:51]\n" \
  "ds_read_b64_tr_b16 v[28:29], v4 offset:51232\n" \
  "ds_add_u32 v3, v2\n" \
  "v_mfma_f32_16x16x32_bf16 v[68:71], v[12:15], a[20:23], v[68:71]\n" \
  "ds_read_b64_tr_b16 v[30:31], v4 offset:52512\n" \
  "s_waitcnt lgkmcnt(8)\n" \
  "v_mfma_f32_16x16x32_bf16 v[52:55], v[16:19], a[16:19], v[52:55]\n" \
  "ds_read_b64_tr_b16 v[12:13], v4 offset:56320\n" \
  "v_mfma_f32_16x16x32_bf16 v[72:75], v[16:19], a[20:23], v[72:75]\n" \
  "ds_read_b64_tr_b16 v[14:15], v4 offset:57600\n" \
  "s_waitcnt lgkmcnt(7)\n" \
  "v_mfma_f32_16x16x32_bf16 v[56:59], v[20:23], a[16:19], v[56:59]\n" \
  "ds_read_b64_tr_b16 v[16:17], v4 offset:56328\n" \
  "v_mfma_f32_16x16x32_bf16 v[76:79], v[20:23], a[20:23], v[76:79]\n" \
  "ds_read_b64_tr_b16 v[18:19], v4 offset:57608\n" \
  "s_waitcnt lgkmcnt(7)\n" \
  "v_mfma_f32_16x16x32_bf16 v[60:63], v[24:27], a[16:19], v[60:63]\n" \
  "ds_read_b64_tr_b16 v[20:21], v4 offset:56336\n" \
  "v_cmp_gt_u32_e32 vcc, 1, v11\n" \
  "v_mfma_f32_16x16x32_bf16 v[80:83], v[24:27], a[20:23], v[80:83]\n" \
  "ds_read_b64_tr_b16 v[22:23], v4 offset:57616\n" \
  "s_waitcnt lgkmcnt(6)\n" \
  "v_mfma_f32_16x16x32_bf16 v[64:67], v[28:31], a[16:19], v[64:67]\n" \
  "ds_read_b64_tr_b16 v[24:25], v4 offset:56344\n" \
  "s_cbranch_vccnz LQ11_s\n" \
  "LQ11_k:\n" \
  "v_mfma_f32_16x16x32_bf16 v[84:87], v[28:31], a[20:23], v[84:87]\n" \
  "ds_read_b64_tr_b16 v[26:27], v4 offset:57624\n" \
  "s_waitcnt lgkmcnt(6)\n" \
  "v_mfma_f32_16x16x32_bf16 v[48:51], v[12:15], a[24:27], v[48:51]\n" \
  "ds_read_b64_tr_b16 v[28:29], v4 offset:56352\n" \
  "v_mfma_f32_16x16x32_bf16 v[68:71], v[12:15], a[28:31], v[68:71]\n" \
  "ds_read_b64_tr_b16 v[30:31], v4 offset:57632\n" \
  "s_waitcnt lgkmcnt(6)\n" \
  "v_mfma_f32_16x16x32_bf16 v[52:55], v[16:19], a[24:27], v[52:55]\n" \
  "ds_read_b64_tr_b16 v[12:13], v5 offset:0\n" \
  "v_mfma_f32_16x16x32_bf16 v[72:75], v[16:19], a[28:31], v[72:75]\n" \
  "ds_read_b64_tr_b16 v[14:15], v5 offset:1280\n" \
  "s_waitcnt lgkmcnt(6)\n" \
  "v_mfma_f32_16x16x32_bf16 v[56:59], v[20:23], a[24:27], v[56:59]\n" \
  "ds_read_b64_tr_b16 v[16:17], v5 offset:8\n" \
  "v_mfma_f32_16x16x32_bf16 v[76:79], v[20:23], a[28:31], v[76:79]\n" \
  "ds_read_b64_tr_b16 v[18:19], v5 offset:1288\n" \
  "s_waitcnt lgkmcnt(6)\n" \
  "v_mfma_f32_16x16x32_bf16 v[60:63], v[24:27], a[24:27], v[60:63]\n" \
  "ds_read_b64_tr_b16 v[20:21], v5 offset:16\n" \
  "v_mfma_f32_16x16x32_bf16 v[80:83], v[24:27], a[28:31], v[80:83]\n" \
  "ds_read_b64_tr_b16 v[22:23], v5 offset:1296\n" \
  "s_waitcnt lgkmcnt(6)\n" \
  "v_mfma_f32_16x16x32_bf16 v[64:67], v[28:31], a[24:27], v[64:67]\n" \
  "ds_read_b64_tr_b16 v[24:25], v5 offset:24\n" \
  "v_mfma_f32_16x16x32_bf16 v[84:87], v[28:31], a[28:31], v[84:87]\n" \
  "ds_read_b64_tr_b16 v[26:27], v5 offset:1304\n" \
  "s_waitcnt lgkmcnt(6)\n" \
  "v_mfma_f32_16x16x32_bf16 v[48:51], v[12:15], a[32:35], v[48:51]\n" \
  "ds_read_b64_tr_b16 v[28:29], v5 offset:32\n" \
  "ds_add_u32 v3, v2\n" \
  "v_mfma_f32_16x16x32_bf16 v[68:71], v[12:15], a[36:39], v[68:71]\n" \
  "ds_read_b64_tr_b16 v[30:31], v5 offset:1312\n" \
  "s_waitcnt lgkmcnt(7)\n" \
  "v_mfma_f32_16x16x32_bf16 v[52:55], v[16:19], a[32:35], v[52:55]\n" \
  "ds_read_b64_tr_b16 v[12:13], v5 offset:5120\n" \
  "v_mfma_f32_16x16x32_bf16 v[72:75], v[16:19], a[36:39], v[72:75]\n" \
  "ds_read_b64_tr_b16 v[14:15], v5 offset:6400\n" \
  "s_waitcnt lgkmcnt(7)\n" \
  "v_mfma_f32_16x16x32_bf16 v[56:59], v[20:23], a[32:35], v[56:59]\n" \
  "ds_read_b64_tr_b16 v[16:17], v5 offset:5128\n" \
  "v_mfma_f32_16x16x32_bf16 v[76:79], v[20:23], a[36:39], v[76:79]\n" \
  "ds_read_b64_tr_b16 v[18:19], v5 offset:6408\n" \
  "s_waitcnt lgkmcnt(7)\n" \
  "v_mfma_f32_16x16x32_bf16 v[60:63], v[24:27], a[32:35], v[60:63]\n" \
  "ds_read_b64_tr_b16 v[20:21], v5 offset:5136\n" \
  "v_mfma_f32_16x16x32_bf16 v[80:83], v[24:27], a[36:39], v[80:83]\n" \
  "ds_read_b64_tr_b16 v[22:23], v5 offset:6416\n" \
  "s_waitcnt lgkmcnt(6)\n" \
  "v_mfma_f32_16x16x32_bf16 v[64:67], v[28:31], a[32:35], v[64:67]\n" \
  "ds_read_b64_tr_b16 v[24:25], v5 offset:5144\n" \
  "v_mfma_f32_16x16x32_bf16 v[84:87], v[28:31], a[36:39], v[84:87]\n" \
  "ds_read_b64_tr_b16 v[26:27], v5 offset:6424\n" \
  "s_waitcnt lgkmcnt(6)\n" \
  "v_mfma_f32_16x16x32_bf16 v[48:51], v[12:15], a[40:43], v[48:51]\n" \
  "ds_read_b64_tr_b16 v[28:29], v5 offset:5152\n" \
  "v_mfma_f32_16x16x32_bf16 v[68:71], v[12:15], a[44:47], v[68:71]\n" \
  "ds_read_b64_tr_b16 v[30:31], v5 offset:6432\n" \
  "s_waitcnt lgkmcnt(6)\n" \
  "v_mfma_f32_16x16x32_bf16 v[52:55], v[16:19], a[40:43], v[52:55]\n" \
  "ds_read_b64_tr_b16 v[12:13], v5 offset:10240\n" \
  "v_mfma_f32_16x16x32_bf16 v[72:75], v[16:19], a[44:47], v[72:75]\n" \
  "ds_read_b64_tr_b16 v[14:15], v5 offset:11520\n" \
  "s_waitcnt lgkmcnt(6)\n" \
  "v_mfma_f32_16x16x32_bf16 v[56:59], v[20:23], a[40:43], v[56:59]\n" \
  "ds_read_b64_tr_b16 v[16:17], v5 offset:10248\n" \
  "v_mfma_f32_16x16x32_bf16 v[76:79], v[20:23], a[44:47], v[76:79]\n" \
  "ds_read_b64_tr_b16 v[18:19], v5 offset:11528\n" \
  "s_waitcnt lgkmcnt(6)\n" \
  "v_mfma_f32_16x16x32_bf16 v[60:63], v[24:27], a[40:43], v[60:63]\n" \
  "ds_read_b64_tr_b16 v[20:21], v5 offset:10256\n" \
  "ds_read_b32 v11, v8 offset:48\n" \
  "v_mfma_f32_16x16x32_bf16 v[80:83], v[24:27], a[44:47], v[80:83]\n" \
  "ds_read_b64_tr_b16 v[22:23], v5 offset:11536\n" \
  "s_waitcnt lgkmcnt(7)\n" \
  "v_mfma_f32_16x16x32_bf16 v[64:67], v[28:31], a[40:43], v[64:67]\n" \
  "ds_read_b64_tr_b16 v[24:25], v5 offset:10264\n" \
  "v_mfma_f32_16x16x32_bf16 v[84:87], v[28:31], a[44:47], v[84:87]\n" \
  "ds_read_b64_tr_b16 v[26:27], v5 offset:11544\n" \
  "s_waitcnt lgkmcnt(7)\n" \
  "v_mfma_f32_16x16x32_bf16 v[48:51], v[12:15], a[48:51], v[48:51]\n" \
  "ds_read_b64_tr_b16 v[28:29], v5 offset:10272\n" \
  "ds_add_u32 v3, v2\n" \
  "v_mfma_f32_16x16x32_bf16 v[68:71], v[12:15], a[52:55], v[68:71]\n" \
  "ds_read_b64_tr_b16 v[30:31], v5 offset:11552\n" \
  "s_waitcnt lgkmcnt(8)\n" \
  "v_mfma_f32_16x16x32_bf16 v[52:55], v[16:19], a[48:51], v[52:55]\n" \
  "ds_read_b64_tr_b16 v[12:13], v5 offset:15360\n" \
  "v_mfma_f32_16x16x32_bf16 v[72:75], v[16:19], a[52:55], v[72:75]\n" \
  "ds_read_b64_tr_b16 v[14:15], v5 offset:16640\n" \
  "s_waitcnt lgkmcnt(7)\n" \
  "v_mfma_f32_16x16x32_bf16 v[56:59], v[20:23], a[48:51], v[56:59]\n" \
  "ds_read_b64_tr_b16 v[16:17], v5 offset:15368\n" \
  "v_mfma_f32_16x16x32_bf16 v[76:79], v[20:23], a[52:55], v[76:79]\n" \
  "ds_read_b64_tr_b16 v[18:19], v5 offset:16648\n" \
  "s_waitcnt lgkmcnt(7)\n" \
  "v_mfma_f32_16x16x32_bf16 v[60:63], v[24:27], a[48:51], v[60:63]\n" \
  "ds_read_b64_tr_b16 v[20:21], v5 offset:15376\n" \
  "v_cmp_gt_u32_e32 vcc, 1, v11\n" \
  "v_mfma_f32_16x16x32_bf16 v[80:83], v[24:27], a[52:55], v[80:83]\n" \
  "ds_read_b64_tr_b16 v[22:23], v5 offset:16656\n" \
  "s_waitcnt lgkmcnt(6)\n" \
  "v_mfma_f32_16x16x32_bf16 v[64:67], v[28:31], a[48:51], v[64:67]\n" \
  "ds_read_b64_tr_b16 v[24:25], v5 offset:15384\n" \
  "s_cbranch_vccnz LQ12_s\n" \
  "LQ12_k:\n" \
  "v_mfma_f32_16x16x32_bf16 v[84:87], v[28:31], a[52:55], v[84:87]\n" \
  "ds_read_b64_tr_b16 v[26:27], v5 offset:16664\n" \
  "s_waitcnt lgkmcnt(6)\n" \
  "v_mfma_f32_16x16x32_bf16 v[48:51], v[12:15], a[56:59], v[48:51]\n" \
  "ds_read_b64_tr_b16 v[28:29], v5 offset:15392\n" \
  "v_mfma_f32_16x16x32_bf16 v[68:71], v[12:15], a[60:63], v[68:71]\n" \
  "ds_read_b64_tr_b16 v[30:31], v5 offset:16672\n" \
  "s_waitcnt lgkmcnt(6)\n" \
  "v_mfma_f32_16x16x32_bf16 v[52:55], v[16:19], a[56:59], v[52:55]\n" \
  "ds_read_b64_tr_b16 v[12:13], v4 offset:0\n" \
  "v_mfma_f32_16x16x32_bf16 v[72:75], v[16:19], a[60:63], v[72:75]\n" \
  "ds_read_b64_tr_b16 v[14:15], v4 offset:1280\n" \
  "s_waitcnt lgkmcnt(6)\n" \
  "v_mfma_f32_16x16x32_bf16 v[56:59], v[20:23], a[56:59], v[56:59]\n" \
  "ds_read_b64_tr_b16 v[16:17], v4 offset:8\n" \
  "v_mfma_f32_16x16x32_bf16 v[76:79], v[20:23], a[60:63], v[76:79]\n" \
  "ds_read_b64_tr_b16 v[18:19], v4 offset:1288\n" \
  "s_waitcnt lgkmcnt(6)\n" \
  "v_mfma_f32_16x16x32_bf16 v[60:63], v[24:27], a[56:59], v[60:63]\n" \
  "ds_read_b64_tr_b16 v[20:21], v4 offset:16\n" \
  "v_mfma_f32_16x16x32_bf16 v[80:83], v[24:27], a[60:63], v[80:83]\n" \
  "ds_read_b64_tr_b16 v[22:23], v4 offset:1296\n" \
  "s_waitcnt lgkmcnt(6)\n" \
  "v_mfma_f32_16x16x32_bf16 v[64:67], v[28:31], a[56:59], v[64:67]\n" \
  "ds_read_b64_tr_b16 v[24:25], v4 offset:24\n" \
  "v_mfma_f32_16x16x32_bf16 v[84:87], v[28:31], a[60:63], v[84:87]\n" \
  "ds_read_b64_tr_b16 v[26:27], v4 offset:1304\n" \
  "s_waitcnt lgkmcnt(6)\n" \
  "v_mfma_f32_16x16x32_bf16 v[48:51], v[12:15], a[64:67], v[48:51]\n" \
  "ds_read_b64_tr_b16 v[28:29], v4 offset:32\n" \
  "ds_add_u32 v3, v2\n" \
  "v_mfma_f32_16x16x32_bf16 v[68:71], v[12:15], a[68:71], v[68:71]\n" \
  "ds_read_b64_tr_b16 v[30:31], v4 offset:1312\n" \
  "s_waitcnt lgkmcnt(7)\n" \
  "v_mfma_f32_16x16x32_bf16 v[52:55], v[16:19], a[64:67], v[52:55]\n" \
  "ds_read_b64_tr_b16 v[12:13], v4 offset:5120\n" \
  "v_mfma_f32_16x16x32_bf16 v[72:75], v[16:19], a[68:71], v[72:75]\n" \
  "ds_read_b64_tr_b16 v[14:15], v4 offset:6400\n" \
  "s_waitcnt lgkmcnt(7)\n" \
  "v_mfma_f32_16x16x32_bf16 v[56:59], v[20:23], a[64:67], v[56:59]\n" \
  "ds_read_b64_tr_b16 v[16:17], v4 offset:5128\n" \
  "v_mfma_f32_16x16x32_bf16 v[76:79], v[20:23], a[68:71], v[76:79]\n" \
  "ds_read_b64_tr_b16 v[18:19], v4 offset:6408\n" \
  "s_waitcnt lgkmcnt(7)\n" \
  "v_mfma_f32_16x16x32_bf16 v[60:63], v[24:27], a[64:67], v[60:63]\n" \
  "ds_read_b64_tr_b16 v[20:21], v4 offset:5136\n" \
  "v_mfma_f32_16x16x32_bf16 v[80:83], v[24:27], a[68:71], v[80:83]\n" \
  "ds_read_b64_tr_b16 v[22:23], v4 offset:6416\n" \
  "s_waitcnt lgkmcnt(6)\n" \
  "v_mfma_f32_16x16x32_bf16 v[64:67], v[28:31], a[64:67], v[64:67]\n" \
  "ds_read_b64_tr_b16 v[24:25], v4 offset:5144\n" \
  "v_mfma_f32_16x16x32_bf16 v[84:87], v[28:31], a[68:71], v[84:87]\n" \
  "ds_read_b64_tr_b16 v[26:27], v4 offset:6424\n" \
  "s_waitcnt lgkmcnt(6)\n" \
  "v_mfma_f32_16x16x32_bf16 v[48:51], v[12:15], a[72:75], v[48:51]\n" \
  "ds_read_b64_tr_b16 v[28:29], v4 offset:5152\n" \
  "v_mfma_f32_16x16x32_bf16 v[68:71], v[12:15], a[76:79], v[68:71]\n" \
  "ds_read_b64_tr_b16 v[30:31], v4 offset:6432\n" \
  "s_waitcnt lgkmcnt(6)\n" \
  "v_mfma_f32_16x16x32_bf16 v[52:55], v[16:19], a[72:75], v[52:55]\n" \
  "ds_read_b64_tr_b16 v[12:13], v4 offset:10240\n" \
  "v_mfma_f32_16x16x32_bf16 v[72:75], v[16:19], a[76:79], v[72:75]\n" \
  "ds_read_b64_tr_b16 v[14:15], v4 offset:11520\n" \
  "s_waitcnt lgkmcnt(6)\n" \
  "v_mfma_f32_16x16x32_bf16 v[56:59], v[20:23], a[72:75], v[56:59]\n" \
  "ds_read_b64_tr_b16 v[16:17], v4 offset:10248\n" \
  "v_mfma_f32_16x16x32_bf16 v[76:79], v[20:23], a[76:79], v[76:79]\n" \
  "ds_read_b64_tr_b16 v[18:19], v4 offset:11528\n" \
  "s_waitcnt lgkmcnt(6)\n" \
  "v_mfma_f32_16x16x32_bf16 v[60:63], v[24:27], a[72:75], v[60:63]\n" \
  "ds_read_b64_tr_b16 v[20:21], v4 offset:10256\n" \
  "ds_read_b32 v11, v8 offset:52\n" \
  "v_mfma_f32_16x16x32_bf16 v[80:83], v[24:27], a[76:79], v[80:83]\n" \
  "ds_read_b64_tr_b16 v[22:23], v4 offset:11536\n" \
  "s_waitcnt lgkmcnt(7)\n" \
  "v_mfma_f32_16x16x32_bf16 v[64:67], v[28:31], a[72:75], v[64:67]\n" \
  "ds_read_b64_tr_b16 v[24:25], v4 offset:10264\n" \
  "v_mfma_f32_16x16x32_bf16 v[84:87], v[28:31], a[76:79], v[84:87]\n" \
  "ds_read_b64_tr_b16 v[26:27], v4 offset:11544\n" \
  "s_waitcnt lgkmcnt(7)\n" \
  "v_mfma_f32_16x16x32_bf16 v[48:51], v[12:15], a[80:83], v[48:51]\n" \
  "ds_read_b64_tr_b16 v[28:29], v4 offset:10272\n" \
  "ds_add_u32 v3, v2\n" \
  "v_mfma_f32_16x16x32_bf16 v[68:71], v[12:15], a[84:87], v[68:71]\n" \
  "ds_read_b64_tr_b16 v[30:31], v4 offset:11552\n" \
  "s_waitcnt lgkmcnt(8)\n" \
  "v_mfma_f32_16x16x32_bf16 v[52:55], v[16:19], a[80:83], v[52:55]\n" \
  "ds_read_b64_tr_b16 v[12:13], v4 offset:15360\n" \
  "v_mfma_f32_16x16x32_bf16 v[72:75], v[16:19], a[84:87], v[72:75]\n" \
  "ds_read_b64_tr_b16 v[14:15], v4 offset:16640\n" \
  "s_waitcnt lgkmcnt(7)\n" \
  "v_mfma_f32_16x16x32_bf16 v[56:59], v[20:23], a[80:83], v[56:59]\n" \
  "ds_read_b64_tr_b16 v[16:17], v4 offset:15368\n" \
  "v_mfma_f32_16x16x32_bf16 v[76:79], v[20:23], a[84:87], v[76:79]\n" \
  "ds_read_b64_tr_b16 v[18:19], v4 offset:16648\n" \
  "s_waitcnt lgkmcnt(7)\n" \
  "v_mfma_f32_16x16x32_bf16 v[60:63], v[24:27], a[80:83], v[60:63]\n" \
  "ds_read_b64_tr_b16 v[20:21], v4 offset:15376\n" \
  "v_cmp_gt_u32_e32 vcc, 1, v11\n" \
  "v_mfma_f32_16x16x32_bf16 v[80:83], v[24:27], a[84:87], v[80:83]\n" \
  "ds_read_b64_tr_b16 v[22:23], v4 offset:16656\n" \
  "s_waitcnt lgkmcnt(6)\n" \
  "v_mfma_f32_16x16x32_bf16 v[64:67], v[28:31], a[80:83], v[64:67]\n" \
  "ds_read_b64_tr_b16 v[24:25], v4 offset:15384\n" \
  "s_cbranch_vccnz LQ13_s\n" \
  "LQ13_k:\n" \
  "v_mfma_f32_16x16x32_bf16 v[84:87], v[28:31], a[84:87], v[84:87]\n" \
  "ds_read_b64_tr_b16 v[26:27], v4 offset:16664\n" \
  "s_waitcnt lgkmcnt(6)\n" \
  "v_mfma_f32_16x16x32_bf16 v[48:51], v[12:15], a[88:91], v[48:51]\n" \
  "ds_read_b64_tr_b16 v[28:29], v4 offset:15392\n" \
  "v_mfma_f32_16x16x32_bf16 v[68:71], v[12:15], a[92:95], v[68:71]\n" \
  "ds_read_b64_tr_b16 v[30:31], v4 offset:16672\n" \
  "s_waitcnt lgkmcnt(6)\n" \
  "v_mfma_f32_16x16x32_bf16 v[52:55], v[16:19], a[88:91], v[52:55]\n" \
  "ds_read_b64_tr_b16 v[12:13], v4 offset:20480\n" \
  "v_mfma_f32_16x16x32_bf16 v[72:75], v[16:19], a[92:95], v[72:75]\n" \
  "ds_read_b64_tr_b16 v[14:15], v4 offset:21760\n" \
  "s_waitcnt lgkmcnt(6)\n" \
  "v_mfma_f32_16x16x32_bf16 v[56:59], v[20:23], a[88:91], v[56:59]\n" \
  "ds_read_b64_tr_b16 v[16:17], v4 offset:20488\n" \
  "v_mfma_f32_16x16x32_bf16 v[76:79], v[20:23], a[92:95], v[76:79]\n" \
  "ds_read_b64_tr_b16 v[18:19], v4 offset:21768\n" \
  "s_waitcnt lgkmcnt(6)\n" \
  "v_mfma_f32_16x16x32_bf16 v[60:63], v[24:27], a[88:91], v[60:63]\n" \
  "ds_read_b64_tr_b16 v[20:21], v4 offset:20496\n" \
  "v_mfma_f32_16x16x32_bf16 v[80:83], v[24:27], a[92:95], v[80:83]\n" \
  "ds_read_b64_tr_b16 v[22:23], v4 offset:21776\n" \
  "s_waitcnt lgkmcnt(6)\n" \
  "v_mfma_f32_16x16x32_bf16 v[64:67], v[28:31], a[88:91], v[64:67]\n" \
  "ds_read_b64_tr_b16 v[24:25], v4 offset:20504\n" \
  "v_mfma_f32_16x16x32_bf16 v[84:87], v[28:31], a[92:95], v[84:87]\n" \
  "ds_read_b64_tr_b16 v[26:27], v4 offset:21784\n" \
  "s_waitcnt lgkmcnt(6)\n" \
  "v_mfma_f32_16x16x32_bf16 v[48:51], v[12:15], a[96:99], v[48:51]\n" \
  "ds_read_b64_tr_b16 v[28:29], v4 offset:20512\n" \
  "ds_add_u32 v3, v2\n" \
  "v_mfma_f32_16x16x32_bf16 v[68:71], v[12:15], a[100:103], v[68:71]\n" \
  "ds_read_b64_tr_b16 v[30:31], v4 offset:21792\n" \
  "s_waitcnt lgkmcnt(7)\n" \
  "v_mfma_f32_16x16x32_bf16 v[52:55], v[16:19], a[96:99], v[52:55]\n" \
  "ds_read_b64_tr_b16 v[12:13], v4 offset:25600\n" \
  "v_mfma_f32_16x16x32_bf16 v[72:75], v[16:19], a[100:103], v[72:75]\n" \
  "ds_read_b64_tr_b16 v[14:15], v4 offset:26880\n" \
  "s_waitcnt lgkmcnt(7)\n" \
  "v_mfma_f32_16x16x32_bf16 v[56:59], v[20:23], a[96:99], v[56:59]\n" \
  "ds_read_b64_tr_b16 v[16:17], v4 offset:25608\n" \
  "v_mfma_f32_16x16x32_bf16 v[76:79], v[20:23], a[100:103], v[76:79]\n" \
  "ds_read_b64_tr_b16 v[18:19], v4 offset:26888\n" \
  "s_waitcnt lgkmcnt(7)\n" \
  "v_mfma_f32_16x16x32_bf16 v[60:63], v[24:27], a[96:99], v[60:63]\n" \
  "ds_read_b64_tr_b16 v[20:21], v4 offset:25616\n" \
  "v_mfma_f32_16x16x32_bf16 v[80:83], v[24:27], a[100:103], v[80:83]\n" \
  "ds_read_b64_tr_b16 v[22:23], v4 offset:26896\n" \
  "s_waitcnt lgkmcnt(6)\n" \
  "v_mfma_f32_16x16x32_bf16 v[64:67], v[28:31], a[96:99], v[64:67]\n" \
  "ds_read_b64_tr_b16 v[24:25], v4 offset:25624\n" \
  "v_mfma_f32_16x16x32_bf16 v[84:87], v[28:31], a[100:103], v[84:87]\n" \
  "ds_read_b64_tr_b16 v[26:27], v4 offset:26904\n" \
  "s_waitcnt lgkmcnt(6)\n" \
  "v_mfma_f32_16x16x32_bf16 v[48:51], v[12:15], a[104:107], v[48:51]\n" \
  "ds_read_b64_tr_b16 v[28:29], v4 offset:25632\n" \
  "v_mfma_f32_16x16x32_bf16 v[68:71], v[12:15], a[108:111], v[68:71]\n" \
  "ds_read_b64_tr_b16 v[30:31], v4 offset:26912\n" \
  "s_waitcnt lgkmcnt(6)\n" \
  "v_mfma_f32_16x16x32_bf16 v[52:55], v[16:19], a[104:107], v[52:55]\n" \
  "ds_read_b64_tr_b16 v[12:13], v4 offset:30720\n" \
  "v_mfma_f32_16x16x32_bf16 v[72:75], v[16:19], a[108:111], v[72:75]\n" \
  "ds_read_b64_tr_b16 v[14:15], v4 offset:32000\n" \
  "s_waitcnt lgkmcnt(6)\n" \
  "v_mfma_f32_16x16x32_bf16 v[56:59], v[20:23], a[104:107], v[56:59]\n" \
  "ds_read_b64_tr_b16 v[16:17], v4 offset:30728\n" \
  "v_mfma_f32_16x16x32_bf16 v[76:79], v[20:23], a[108:111], v[76:79]\n" \
  "ds_read_b64_tr_b16 v[18:19], v4 offset:32008\n" \
  "s_waitcnt lgkmcnt(6)\n" \
  "v_mfma_f32_16x16x32_bf16 v[60:63], v[24:27], a[104:107], v[60:63]\n" \
  "ds_read_b64_tr_b16 v[20:21], v4 offset:30736\n" \
  "ds_read_b32 v11, v8 offset:56\n" \
  "v_mfma_f32_16x16x32_bf16 v[80:83], v[24:27], a[108:111], v[80:83]\n" \
  "ds_read_b64_tr_b16 v[22:23], v4 offset:32016\n" \
  "s_waitcnt lgkmcnt(7)\n" \
  "v_mfma_f32_16x16x32_bf16 v[64:67], v[28:31], a[104:107], v[64:67]\n" \
  "ds_read_b64_tr_b16 v[24:25], v4 offset:30744\n" \
  "v_mfma_f32_16x16x32_bf16 v[84:87], v[28:31], a[108:111], v[84:87]\n" \
  "ds_read_b64_tr_b16 v[26:27], v4 offset:32024\n" \
  "s_waitcnt lgkmcnt(7)\n" \
  "v_mfma_f32_16x16x32_bf16 v[48:51], v[12:15], a[112:115], v[48:51]\n" \
  "ds_read_b64_tr_b16 v[28:29], v4 offset:30752\n" \
  "ds_add_u32 v3, v2\n" \
  "v_mfma_f32_16x16x32_bf16 v[68:71], v[12:15], a[116:119], v[68:71]\n" \
  "ds_read_b64_tr_b16 v[30:31], v4 offset:32032\n" \
  "s_waitcnt lgkmcnt(8)\n" \
  "v_mfma_f32_16x16x32_bf16 v[52:55], v[16:19], a[112:115], v[52:55]\n" \
  "ds_read_b64_tr_b16 v[12:13], v4 offset:35840\n" \
  "v_mfma_f32_16x16x32_bf16 v[72:75], v[16:19], a[116:119], v[72:75]\n" \
  "ds_read_b64_tr_b16 v[14:15], v4 offset:37120\n" \
  "s_waitcnt lgkmcnt(7)\n" \
  "v_mfma_f32_16x16x32_bf16 v[56:59], v[20:23], a[112:115], v[56:59]\n" \
  "ds_read_b64_tr_b16 v[16:17], v4 offset:35848\n" \
  "v_mfma_f32_16x16x32_bf16 v[76:79], v[20:23], a[116:119], v[76:79]\n" \
  "ds_read_b64_tr_b16 v[18:19], v4 offset:37128\n" \
  "s_waitcnt lgkmcnt(7)\n" \
  "v_mfma_f32_16x16x32_bf16 v[60:63], v[24:27], a[112:115], v[60:63]\n" \
  "ds_read_b64_tr_b16 v[20:21], v4 offset:35856\n" \
  "v_cmp_gt_u32_e32 vcc, 1, v11\n" \
  "v_mfma_f32_16x16x32_bf16 v[80:83], v[24:27], a[116:119], v[80:83]\n" \
  "ds_read_b64_tr_b16 v[22:23], v4 offset:37136\n" \
  "s_waitcnt lgkmcnt(6)\n" \
  "v_mfma_f32_16x16x32_bf16 v[64:67], v[28:31], a[112:115], v[64:67]\n" \
  "ds_read_b64_tr_b16 v[24:25], v4 offset:35864\n" \
  "s_cbranch_vccnz LQ14_s\n" \
  "LQ14_k:\n" \
  "v_mfma_f32_16x16x32_bf16 v[84:87], v[28:31], a[116:119], v[84:87]\n" \
  "ds_read_b64_tr_b16 v[26:27], v4 offset:37144\n" \
  "s_waitcnt lgkmcnt(6)\n" \
  "v_mfma_f32_16x16x32_bf16 v[48:51], v[12:15], a[120:123], v[48:51]\n" \
  "ds_read_b64_tr_b16 v[28:29], v4 offset:35872\n" \
  "v_mfma_f32_16x16x32_bf16 v[68:71], v[12:15], a[124:127], v[68:71]\n" \
  "ds_read_b64_tr_b16 v[30:31], v4 offset:37152\n" \
  "s_waitcnt lgkmcnt(6)\n" \
  "v_mfma_f32_16x16x32_bf16 v[52:55], v[16:19], a[120:123], v[52:55]\n" \
  "ds_read_b64_tr_b16 v[12:13], v4 offset:40960\n" \
  "v_mfma_f32_16x16x32_bf16 v[72:75], v[16:19], a[124:127], v[72:75]\n" \
  "ds_read_b64_tr_b16 v[14:15], v4 offset:42240\n" \
  "s_waitcnt lgkmcnt(6)\n" \
  "v_mfma_f32_16x16x32_bf16 v[56:59], v[20:23], a[120:123], v[56:59]\n" \
  "ds_read_b64_tr_b16 v[16:17], v4 offset:40968\n" \
  "v_mfma_f32_16x16x32_bf16 v[76:79], v[20:23], a[124:127], v[76:79]\n" \
  "ds_read_b64_tr_b16 v[18:19], v4 offset:42248\n" \
  "s_waitcnt lgkmcnt(6)\n" \
  "v_mfma_f32_16x16x32_bf16 v[60:63], v[24:27], a[120:123], v[60:63]\n" \
  "ds_read_b64_tr_b16 v[20:21], v4 offset:40976\n" \
  "ds_read_b32 v11, v8 offset:60\n" \
  "v_mfma_f32_16x16x32_bf16 v[80:83], v[24:27], a[124:127], v[80:83]\n" \
  "ds_read_b64_tr_b16 v[22:23], v4 offset:42256\n" \
  "s_waitcnt lgkmcnt(7)\n" \
  "v_mfma_f32_16x16x32_bf16 v[64:67], v[28:31], a[120:123], v[64:67]\n" \
  "ds_read_b64_tr_b16 v[24:25], v4 offset:40984\n" \
  "v_mfma_f32_16x16x32_bf16 v[84:87], v[28:31], a[124:127], v[84:87]\n" \
  "ds_read_b64_tr_b16 v[26:27], v4 offset:42264\n" \
  "s_waitcnt lgkmcnt(7)\n" \
  "v_mfma_f32_16x16x32_bf16 v[48:51], v[12:15], v[96:99], v[48:51]\n" \
  "ds_read_b64_tr_b16 v[28:29], v4 offset:40992\n" \
  "ds_add_u32 v3, v2\n" \
  "v_mfma_f32_16x16x32_bf16 v[68:71], v[12:15], v[100:103], v[68:71]\n" \
  "ds_read_b64_tr_b16 v[30:31], v4 offset:42272\n" \
  "s_waitcnt lgkmcnt(8)\n" \
  "v_mfma_f32_16x16x32_bf16 v[52:55], v[16:19], v[96:99], v[52:55]\n" \
  "ds_read_b64_tr_b16 v[12:13], v4 offset:46080\n" \
  "v_mfma_f32_16x16x32_bf16 v[72:75], v[16:19], v[100:103], v[72:75]\n" \
  "ds_read_b64_tr_b16 v[14:15], v4 offset:47360\n" \
  "s_waitcnt lgkmcnt(7)\n" \
  "v_mfma_f32_16x16x32_bf16 v[56:59], v[20:23], v[96:99], v[56:59]\n" \
  "ds_read_b64_tr_b16 v[16:17], v4 offset:46088\n" \
  "v_mfma_f32_16x16x32_bf16 v[76:79], v[20:23], v[100:103], v[76:79]\n" \
  "ds_read_b64_tr_b16 v[18:19], v4 offset:47368\n" \
  "s_waitcnt lgkmcnt(7)\n" \
  "v_mfma_f32_16x16x32_bf16 v[60:63], v[24:27], v[96:99], v[60:63]\n" \
  "ds_read_b64_tr_b16 v[20:21], v4 offset:46096\n" \
  "v_cmp_gt_u32_e32 vcc, 1, v11\n" \
  "v_mfma_f32_16x16x32_bf16 v[80:83], v[24:27], v[100:103], v[80:83]\n" \
  "ds_read_b64_tr_b16 v[22:23], v4 offset:47376\n" \
  "s_waitcnt lgkmcnt(6)\n" \
  "v_mfma_f32_16x16x32_bf16 v[64:67], v[28:31], v[96:99], v[64:67]\n" \
  "ds_read_b64_tr_b16 v[24:25], v4 offset:46104\n" \
  "s_cbranch_vccnz LQ15_s\n" \
  "LQ15_k:\n" \
  "v_mfma_f32_16x16x32_bf16 v[84:87], v[28:31], v[100:103], v[84:87]\n" \
  "ds_read_b64_tr_b16 v[26:27], v4 offset:47384\n" \
  "s_waitcnt lgkmcnt(6)\n" \
  "v_mfma_f32_16x16x32_bf16 v[48:51], v[12:15], v[104:107], v[48:51]\n" \
  "ds_read_b64_tr_b16 v[28:29], v4 offset:46112\n" \
  "ds_read_b64_tr_b16 v[32:33], v9 offset:0\n" \
  "v_mfma_f32_16x16x32_bf16 v[68:71], v[12:15], v[108:111], v[68:71]\n" \
  "ds_read_b64_tr_b16 v[30:31], v4 offset:47392\n" \
  "s_waitcnt lgkmcnt(7)\n" \
  "v_mfma_f32_16x16x32_bf16 v[52:55], v[16:19], v[104:107], v[52:55]\n" \
  "ds_read_b64_tr_b16 v[12:13], v4 offset:51200\n" \
  "ds_read_b64_tr_b16 v[34:35], v9 offset:160\n" \
  "v_mfma_f32_16x16x32_bf16 v[72:75], v[16:19], v[108:111], v[72:75]\n" \
  "ds_read_b64_tr_b16 v[14:15], v4 offset:52480\n" \
  "s_waitcnt lgkmcnt(8)\n" \
  "v_mfma_f32_16x16x32_bf16 v[56:59], v[20:23], v[104:107], v[56:59]\n" \
  "ds_read_b64_tr_b16 v[16:17], v4 offset:51208\n" \
  "ds_read_b64_tr_b16 v[36:37], v9 offset:320\n" \
  "v_mfma_f32_16x16x32_bf16 v[76:79], v[20:23], v[108:111], v[76:79]\n" \
  "ds_read_b64_tr_b16 v[18:19], v4 offset:52488\n" \
  "s_waitcnt lgkmcnt(9)\n" \
  "v_mfma_f32_16x16x32_bf16 v[60:63], v[24:27], v[104:107], v[60:63]\n" \
  "ds_read_b64_tr_b16 v[20:21], v4 offset:51216\n" \
  "ds_read_b64_tr_b16 v[38:39], v9 offset:480\n" \
  "v_mfma_f32_16x16x32_bf16 v[80:83], v[24:27], v[108:111], v[80:83]\n" \
  "ds_read_b64_tr_b16 v[22:23], v4 offset:52496\n" \
  "s_waitcnt lgkmcnt(9)\n" \
  "v_mfma_f32_16x16x32_bf16 v[64:67], v[28:31], v[104:107], v[64:67]\n" \
  "ds_read_b64_tr_b16 v[24:25], v4 offset:51224\n" \
  "ds_read_b64_tr_b16 v[40:41], v9 offset:640\n" \
  "v_mfma_f32_16x16x32_bf16 v[84:87], v[28:31], v[108:111], v[84:87]\n" \
  "ds_read_b64_tr_b16 v[26:27], v4 offset:52504\n" \
  "s_waitcnt lgkmcnt(9)\n" \
  "v_mfma_f32_16x16x32_bf16 v[48:51], v[12:15], v[112:115], v[48:51]\n" \
  "ds_read_b64_tr_b16 v[28:29], v4 offset:51232\n" \
  "ds_add_u32 v3, v2\n" \
  "v_mfma_f32_16x16x32_bf16 v[68:71], v[12:15], v[116:119], v[68:71]\n" \
  "ds_read_b64_tr_b16 v[30:31], v4 offset:52512\n" \
  "s_waitcnt lgkmcnt(9)\n" \
  "v_mfma_f32_16x16x32_bf16 v[52:55], v[16:19], v[112:115], v[52:55]\n" \
  "ds_read_b64_tr_b16 v[12:13], v4 offset:56320\n" \
  "ds_read_b64_tr_b16 v[88:89], v0 offset:0\n" \
  "v_mfma_f32_16x16x32_bf16 v[72:75], v[16:19], v[116:119], v[72:75]\n" \
  "ds_read_b64_tr_b16 v[14:15], v4 offset:57600\n" \
  "s_waitcnt lgkmcnt(9)\n" \
  "v_mfma_f32_16x16x32_bf16 v[56:59], v[20:23], v[112:115], v[56:59]\n" \
  "ds_read_b64_tr_b16 v[16:17], v4 offset:56328\n" \
  "ds_read_b64_tr_b16 v[90:91], v0 offset:160\n" \
  "v_mfma_f32_16x16x32_bf16 v[76:79], v[20:23], v[116:119], v[76:79]\n" \
  "ds_read_b64_tr_b16 v[18:19], v4 offset:57608\n" \
  "s_waitcnt lgkmcnt(9)\n" \
  "v_mfma_f32_16x16x32_bf16 v[60:63], v[24:27], v[112:115], v[60:63]\n" \
  "ds_read_b64_tr_b16 v[20:21], v4 offset:56336\n" \
  "ds_read_b64_tr_b16 v[92:93], v0 offset:800\n" \
  "v_mfma_f32_16x16x32_bf16 v[80:83], v[24:27], v[116:119], v[80:83]\n" \
  "ds_read_b64_tr_b16 v[22:23], v4 offset:57616\n" \
  "s_waitcnt lgkmcnt(9)\n" \
  "v_mfma_f32_16x16x32_bf16 v[64:67], v[28:31], v[112:115], v[64:67]\n" \
  "ds_read_b64_tr_b16 v[24:25], v4 offset:56344\n" \
  "ds_read_b64_tr_b16 v[94:95], v0 offset:960\n" \
  "v_mfma_f32_16x16x32_bf16 v[84:87], v[28:31], v[116:119], v[84:87]\n" \
  "ds_read_b64_tr_b16 v[26:27], v4 offset:57624\n" \
  "s_waitcnt lgkmcnt(9)\n" \
  "v_mfma_f32_16x16x32_bf16 v[48:51], v[12:15], v[120:123], v[48:51]\n" \
  "ds_read_b64_tr_b16 v[28:29], v4 offset:56352\n" \
  "ds_read_b64_tr_b16 v[42:43], v0 offset:320\n" \
  "v_mfma_f32_16x16x32_bf16 v[68:71], v[12:15], v[124:127], v[68:71]\n" \
  "ds_read_b64_tr_b16 v[30:31], v4 offset:57632\n" \
  "s_waitcnt lgkmcnt(9)\n" \
  "v_mfma_f32_16x16x32_bf16 v[52:55], v[16:19], v[120:123], v[52:55]\n" \
  "ds_read_b64_tr_b16 v[44:45], v0 offset:480\n" \
  "v_mfma_f32_16x16x32_bf16 v[72:75], v[16:19], v[124:127], v[72:75]\n" \
  "s_waitcnt lgkmcnt(7)\n" \
  "v_mfma_f32_16x16x32_bf16 v[56:59], v[20:23], v[120:123], v[56:59]\n" \
  "ds_read_b64_tr_b16 v[46:47], v0 offset:640\n" \
  "v_mfma_f32_16x16x32_bf16 v[76:79], v[20:23], v[124:127], v[76:79]\n" \
  "s_waitcnt lgkmcnt(5)\n" \
  "v_mfma_f32_16x16x32_bf16 v[60:63], v[24:27], v[120:123], v[60:63]\n" \
  "v_mfma_f32_16x16x32_bf16 v[80:83], v[24:27], v[124:127], v[80:83]\n" \
  "s_waitcnt lgkmcnt(2)\n" \
  "v_mfma_f32_16x16x32_bf16 v[64:67], v[28:31], v[120:123], v[64:67]\n" \
  "v_mfma_f32_16x16x32_bf16 v[84:87], v[28:31], v[124:127], v[84:87]\n" \
  "ds_add_u32 v3, v2\n" \
  MID(2) \
  "s_cmp_lg_u32 s74, 0\n" \
  "s_cbranch_scc1 LFQ\n" \
  "s_nop 2\n" \
  "v_cvt_pk_bf16_f32 v48, v48, v49\n" \
  "v_cvt_pk_bf16_f32 v49, v50, v51\n" \
  "v_cvt_pk_bf16_f32 v50, v52, v53\n" \
  "v_cvt_pk_bf16_f32 v51, v54, v55\n" \
  "ds_write_b64 v10, v[48:49] offset:0\n" \
  "v_cvt_pk_bf16_f32 v52, v56, v57\n" \
  "v_cvt_pk_bf16_f32 v53, v58, v59\n" \
  "ds_write_b64 v10, v[50:51] offset:8\n" \
  "v_cvt_pk_bf16_f32 v54, v60, v61\n" \
  "v_cvt_pk_bf16_f32 v55, v62, v63\n" \
  "ds_write_b64 v10, v[52:53] offset:16\n" \
  "v_cvt_pk_bf16_f32 v56, v64, v65\n" \
  "v_cvt_pk_bf16_f32 v57, v66, v67\n" \
  "ds_write_b64 v10, v[54:55] offset:24\n" \
  "v_cvt_pk_bf16_f32 v58, v68, v69\n" \
  "v_cvt_pk_bf16_f32 v59, v70, v71\n" \
  "ds_write_b64 v10, v[56:57] offset:32\n" \
  "v_cvt_pk_bf16_f32 v60, v72, v73\n" \
  "v_cvt_pk_bf16_f32 v61, v74, v75\n" \
  "ds_write_b64 v10, v[58:59] offset:2688\n" \
  "v_cvt_pk_bf16_f32 v62, v76, v77\n" \
  "v_cvt_pk_bf16_f32 v63, v78, v79\n" \
  "ds_write_b64 v10, v[60:61] offset:2696\n" \
  "v_cvt_pk_bf16_f32 v64, v80, v81\n" \
  "v_cvt_pk_bf16_f32 v65, v82, v83\n" \
  "ds_write_b64 v10, v[62:63] offset:2704\n" \
  "v_cvt_pk_bf16_f32 v66, v84, v85\n" \
  "v_cvt_pk_bf16_f32 v67, v86, v87\n" \
  "ds_write_b64 v10, v[64:65] offset:2712\n" \
  "ds_write_b64 v10, v[66:67] offset:2720\n" \
  "ds_add_u32 v3, v2 offset:16\n" \
  "s_branch LEND\n" \
  "LFQ:\n" \
  "ds_read_b64_tr_b16 v[12:13], v0 offset:1120\n" \
  "ds_read_b64_tr_b16 v[14:15], v0 offset:1280\n" \
  "ds_read_b64_tr_b16 v[16:17], v0 offset:1440\n" \
  "v_cvt_pk_bf16_f32 v108, v48, v49\n" \
  "v_cvt_pk_bf16_f32 v120, v68, v69\n" \
  "v_cvt_pk_bf16_f32 v109, v50, v51\n" \
  "v_cvt_pk_bf16_f32 v121, v70, v71\n" \
  "v_cvt_pk_bf16_f32 v110, v52, v53\n" \
  "v_cvt_pk_bf16_f32 v122, v72, v73\n" \
  "v_cvt_pk_bf16_f32 v111, v54, v55\n" \
  "v_cvt_pk_bf16_f32 v123, v74, v75\n" \
  "v_cvt_pk_bf16_f32 v112, v56, v57\n" \
  "v_cvt_pk_bf16_f32 v124, v76, v77\n" \
  "v_cvt_pk_bf16_f32 v113, v58, v59\n" \
  "v_cvt_pk_bf16_f32 v125, v78, v79\n" \
  "v_cvt_pk_bf16_f32 v114, v60, v61\n" \
  "v_cvt_pk_bf16_f32 v126, v80, v81\n" \
  "v_cvt_pk_bf16_f32 v115, v62, v63\n" \
  "v_cvt_pk_bf16_f32 v127, v82, v83\n" \
  "v_cvt_pk_bf16_f32 v118, v64, v65\n" \
  "v_cvt_pk_bf16_f32 v106, v84, v85\n" \
  "v_cvt_pk_bf16_f32 v119, v66, v67\n" \
  "v_cvt_pk_bf16_f32 v107, v86, v87\n" \
  "v_mov_b32_e32 v116, 0\n" \
  "v_mov_b32_e32 v117, 0\n" \
  "v_mov_b32_e32 v104, 0\n" \
  "v_mov_b32_e32 v105, 0\n" \
  "s_waitcnt lgkmcnt(3)\n" \
  "v_mfma_f32_16x16x32_bf16 v[64:67], v[32:35], v[108:111], 0\n" \
  "v_mfma_f32_16x16x32_bf16 v[68:71], v[32:35], v[120:123], 0\n" \
  "v_mfma_f32_16x16x32_bf16 v[64:67], v[36:39], v[112:115], v[64:67]\n" \
  "v_permlane16_swap_b32_e32 v108, v120\n" \
  "v_permlane16_swap_b32_e32 v109, v121\n" \
  "v_mfma_f32_16x16x32_bf16 v[68:71], v[36:39], v[124:127], v[68:71]\n" \
  "v_permlane16_swap_b32_e32 v110, v122\n" \
  "v_permlane16_swap_b32_e32 v111, v123\n" \
  "v_mfma_f32_16x16x32_bf16 v[64:67], v[38:41], v[116:119], v[64:67]\n" \
  "v_permlane16_swap_b32_e32 v112, v124\n" \
  "v_permlane16_swap_b32_e32 v113, v125\n" \
  "v_mfma_f32_16x16x32_bf16 v[68:71], v[38:41], v[104:107], v[68:71]\n" \
  "v_permlane16_swap_b32_e32 v114, v126\n" \
  "v_permlane16_swap_b32_e32 v115, v127\n" \
  "v_mfma_f32_32x32x16_bf16 v[48:63], v[88:91], v[108:111], 0\n" \
  "v_permlane16_swap_b32_e32 v118, v106\n" \
  "v_permlane16_swap_b32_e32 v119, v107\n" \
  "v_mfma_f32_32x32x16_bf16 v[72:87], v[92:95], v[120:123], 0\n" \
  "s_nop 3\n" \
  "v_permlane16_swap_b32_e32 v64, v68\n" \
  "v_permlane16_swap_b32_e32 v65, v69\n" \
  "v_mfma_f32_32x32x16_bf16 v[48:63], v[42:45], v[112:115], v[48:63]\n" \
  "v_permlane16_swap_b32_e32 v66, v70\n" \
  "v_permlane16_swap_b32_e32 v67, v71\n" \
  "s_waitcnt lgkmcnt(0)\n" \
  "v_mfma_f32_32x32x16_bf16 v[72:87], v[12:15], v[124:127], v[72:87]\n" \
  "v_mbcnt_lo_u32_b32 v5, -1, 0\n" \
  "v_mbcnt_hi_u32_b32 v5, -1, v5\n" \
  "v_lshlrev_b32_e32 v5, 4, v5\n" \
  "v_add_u32_e32 v5, 160256, v5\n" \
  "v_mfma_f32_32x32x16_bf16 v[48:63], v[44:47], v[116:119], v[48:63]\n" \
  "v_mfma_f32_32x32x16_bf16 v[72:87], v[14:17], v[104:107], v[72:87]\n" \
  "s_nop 7\n" \
  "v_max_f32_e32 v40, v64, v65\n" \
  "v_min_f32_e32 v41, v64, v65\n" \
  "v_max_f32_e32 v42, v66, v67\n" \
  "v_min_f32_e32 v43, v66, v67\n" \
  "ds_read_b128 v[88:91], v5\n" \
  "v_max3_f32 v40, v40, v48, v49\n" \
  "v_min3_f32 v41, v41, v48, v49\n" \
  "v_max3_f32 v42, v42, v50, v51\n" \
  "v_min3_f32 v43, v43, v50, v51\n" \
  "ds_read_b128 v[92:95], v5 offset:1024\n" \
  "v_max3_f32 v40, v40, v52, v53\n" \
  "v_min3_f32 v41, v41, v52, v53\n" \
  "v_max3_f32 v42, v42, v54, v55\n" \
  "v_min3_f32 v43, v43, v54, v55\n" \
  "ds_read_b32 v5, v5 offset:2048\n" \
  "v_max3_f32 v40, v40, v56, v57\n" \
  "v_min3_f32 v41, v41, v56, v57\n" \
  "v_max3_f32 v42, v42, v58, v59\n" \
  "v_min3_f32 v43, v43, v58, v59\n" \
  "v_max3_f32 v40, v40, v60, v61\n" \
  "v_min3_f32 v41, v41, v60, v61\n" \
  "v_max3_f32 v42, v42, v62, v63\n" \
  "v_min3_f32 v43, v43, v62, v63\n" \
  "v_max_f32_e32 v44, v40, v42\n" \
  "v_min_f32_e32 v46, v41, v43\n" \
  "v_mul_f32_e32 v44, s70, v44\n" \
  "v_mul_f32_e32 v46, s70, v46\n" \
  "v_pk_fma_f32 v[20:21], v[48:49], s[70:71], v[46:47] op_sel_hi:[1,0,0] neg_lo:[1,0,0] neg_hi:[1,0,0]\n" \
  "v_pk_fma_f32 v[48:49], v[48:49], s[70:71], v[44:45] op_sel_hi:[1,0,0] neg_lo:[0,0,1] neg_hi:[0,0,1]\n" \
  "v_pk_fma_f32 v[22:23], v[50:51], s[70:71], v[46:47] op_sel_hi:[1,0,0] neg_lo:[1,0,0] neg_hi:[1,0,0]\n" \
  "v_pk_fma_f32 v[50:51], v[50:51], s[70:71], v[44:45] op_sel_hi:[1,0,0] neg_lo:[0,0,1] neg_hi:[0,0,1]\n" \
  "v_pk_fma_f32 v[24:25], v[52:53], s[70:71], v[46:47] op_sel_hi:[1,0,0] neg_lo:[1,0,0] neg_hi:[1,0,0]\n" \
  "v_pk_fma_f32 v[52:53], v[52:53], s[70:71], v[44:45] op_sel_hi:[1,0,0] neg_lo:[0,0,1] neg_hi:[0,0,1]\n" \
  "v_pk_fma_f32 v[26:27], v[54:55], s[70:71], v[46:47] op_sel_hi:[1,0,0] neg_lo:[1,0,0] neg_hi:[1,0,0]\n" \
  "v_pk_fma_f32 v[54:55], v[54:55], s[70:71], v[44:45] op_sel_hi:[1,0,0] neg_lo:[0,0,1] neg_hi:[0,0,1]\n" \
  "v_pk_fma_f32 v[28:29], v[56:57], s[70:71], v[46:47] op_sel_hi:[1,0,0] neg_lo:[1,0,0] neg_hi:[1,0,0]\n" \
  "v_pk_fma_f32 v[56:57], v[56:57], s[70:71], v[44:45] op_sel_hi:[1,0,0] neg_lo:[0,0,1] neg_hi:[0,0,1]\n" \
  "v_pk_fma_f32 v[30:31], v[58:59], s[70:71], v[46:47] op_sel_hi:[1,0,0] neg_lo:[1,0,0] neg_hi:[1,0,0]\n" \
  "v_pk_fma_f32 v[58:59], v[58:59], s[70:71], v[44:45] op_sel_hi:[1,0,0] neg_lo:[0,0,1] neg_hi:[0,0,1]\n" \
  "v_pk_fma_f32 v[32:33], v[60:61], s[70:71], v[46:47] op_sel_hi:[1,0,0] neg_lo:[1,0,0] neg_hi:[1,0,0]\n" \
  "v_pk_fma_f32 v[60:61], v[60:61], s[70:71], v[44:45] op_sel_hi:[1,0,0] neg_lo:[0,0,1] neg_hi:[0,0,1]\n" \
  "v_pk_fma_f32 v[34:35], v[62:63], s[70:71], v[46:47] op_sel_hi:[1,0,0] neg_lo:[1,0,0] neg_hi:[1,0,0]\n" \
  "v_pk_fma_f32 v[62:63], v[62:63], s[70:71], v[44:45] op_sel_hi:[1,0,0] neg_lo:[0,0,1] neg_hi:[0,0,1]\n" \
  "v_pk_fma_f32 v[36:37], v[64:65], s[70:71], v[46:47] op_sel_hi:[1,0,0] neg_lo:[1,0,0] neg_hi:[1,0,0]\n" \
  "v_pk_fma_f32 v[64:65], v[64:65], s[70:71], v[44:45] op_sel_hi:[1,0,0] neg_lo:[0,0,1] neg_hi:[0,0,1]\n" \
  "v_pk_fma_f32 v[38:39], v[66:67], s[70:71], v[46:47] op_sel_hi:[1,0,0] neg_lo:[1,0,0] neg_hi:[1,0,0]\n" \
  "v_pk_fma_f32 v[66:67], v[66:67], s[70:71], v[44:45] op_sel_hi:[1,0,0] neg_lo:[0,0,1] neg_hi:[0,0,1]\n" \
  "v_exp_f32_e32 v48, v48\n" \
  "v_exp_f32_e32 v20, v20\n" \
  "v_exp_f32_e32 v49, v49\n" \
  "v_exp_f32_e32 v21, v21\n" \
  "v_exp_f32_e32 v50, v50\n" \
  "v_exp_f32_e32 v22, v22\n" \
  "v_exp_f32_e32 v51, v51\n" \
  "v_exp_f32_e32 v23, v23\n" \
  "v_exp_f32_e32 v52, v52\n" \
  "v_exp_f32_e32 v24, v24\n" \
  "v_exp_f32_e32 v53, v53\n" \
  "v_exp_f32_e32 v25, v25\n" \
  "v_exp_f32_e32 v54, v54\n" \
  "v_exp_f32_e32 v26, v26\n" \
  "v_exp_f32_e32 v55, v55\n" \
  "v_exp_f32_e32 v27, v27\n" \
  "v_exp_f32_e32 v56, v56\n" \
  "v_exp_f32_e32 v28, v28\n" \
  "v_exp_f32_e32 v57, v57\n" \
  "v_exp_f32_e32 v29, v29\n" \
  "v_exp_f32_e32 v58, v58\n" \
  "v_exp_f32_e32 v30, v30\n" \
  "v_exp_f32_e32 v59, v59\n" \
  "v_exp_f32_e32 v31, v31\n" \
  "v_exp_f32_e32 v60, v60\n" \
  "v_exp_f32_e32 v32, v32\n" \
  "v_exp_f32_e32 v61, v61\n" \
  "v_exp_f32_e32 v33, v33\n" \
  "v_exp_f32_e32 v62, v62\n" \
  "v_exp_f32_e32 v34, v34\n" \
  "v_exp_f32_e32 v63, v63\n" \
  "v_exp_f32_e32 v35, v35\n" \
  "v_exp_f32_e32 v64, v64\n" \
  "v_exp_f32_e32 v36, v36\n" \
  "v_exp_f32_e32 v65, v65\n" \
  "v_exp_f32_e32 v37, v37\n" \
  "v_exp_f32_e32 v66, v66\n" \
  "v_exp_f32_e32 v38, v38\n" \
  "v_exp_f32_e32 v67, v67\n" \
  "v_exp_f32_e32 v39, v39\n" \
  "v_pk_add_f32 v[44:45], v[48:49], v[50:51]\n" \
  "v_pk_add_f32 v[46:47], v[20:21], v[22:23]\n" \
  "v_pk_add_f32 v[44:45], v[44:45], v[52:53]\n" \
  "v_pk_add_f32 v[46:47], v[46:47], v[24:25]\n" \
  "v_pk_add_f32 v[44:45], v[44:45], v[54:55]\n" \
  "v_pk_add_f32 v[46:47], v[46:47], v[26:27]\n" \
  "v_pk_add_f32 v[44:45], v[44:45], v[56:57]\n" \
  "v_pk_add_f32 v[46:47], v[46:47], v[28:29]\n" \
  "v_pk_add_f32 v[44:45], v[44:45], v[58:59]\n" \
  "v_pk_add_f32 v[46:47], v[46:47], v[30:31]\n" \
  "v_pk_add_f32 v[44:45], v[44:45], v[60:61]\n" \
  "v_pk_add_f32 v[46:47], v[46:47], v[32:33]\n" \
  "v_pk_add_f32 v[44:45], v[44:45], v[62:63]\n" \
  "v_pk_add_f32 v[46:47], v[46:47], v[34:35]\n" \
  "v_pk_add_f32 v[44:45], v[44:45], v[64:65]\n" \
  "v_pk_add_f32 v[46:47], v[46:47], v[36:37]\n" \
  "v_pk_add_f32 v[44:45], v[44:45], v[66:67]\n" \
  "v_pk_add_f32 v[46:47], v[46:47], v[38:39]\n" \
  "v_add_f32_e32 v6, v44, v45\n" \
  "v_add_f32_e32 v46, v46, v47\n" \
  "v_rcp_f32_e32 v46, v46\n" \
  "v_cvt_pk_bf16_f32 v10, v48, v49\n" \
  "v_mul_f32_e32 v46, v46, v6\n" \
  "v_pk_mul_f32 v[20:21], v[20:21], v[46:47] op_sel_hi:[1,0]\n" \
  "v_pk_mul_f32 v[22:23], v[22:23], v[46:47] op_sel_hi:[1,0]\n" \
  "v_pk_mul_f32 v[24:25], v[24:25], v[46:47] op_sel_hi:[1,0]\n" \
  "v_pk_mul_f32 v[26:27], v[26:27], v[46:47] op_sel_hi:[1,0]\n" \
  "v_pk_mul_f32 v[28:29], v[28:29], v[46:47] op_sel_hi:[1,0]\n" \
  "v_pk_mul_f32 v[30:31], v[30:31], v[46:47] op_sel_hi:[1,0]\n" \
  "v_pk_mul_f32 v[32:33], v[32:33], v[46:47] op_sel_hi:[1,0]\n" \
  "v_pk_mul_f32 v[34:35], v[34:35], v[46:47] op_sel_hi:[1,0]\n" \
  "v_pk_mul_f32 v[36:37], v[36:37], v[46:47] op_sel_hi:[1,0]\n" \
  "v_pk_mul_f32 v[38:39], v[38:39], v[46:47] op_sel_hi:[1,0]\n" \
  "v_cvt_pk_bf16_f32 v11, v50, v51\n" \
  "v_cvt_pk_bf16_f32 v12, v52, v53\n" \
  "v_cvt_pk_bf16_f32 v13, v54, v55\n" \
  "v_cvt_pk_bf16_f32 v14, v56, v57\n" \
  "v_cvt_pk_bf16_f32 v15, v58, v59\n" \
  "v_cvt_pk_bf16_f32 v16, v60, v61\n" \
  "v_cvt_pk_bf16_f32 v17, v62, v63\n" \
  "v_cvt_pk_bf16_f32 v18, v64, v65\n" \
  "v_cvt_pk_bf16_f32 v19, v66, v67\n" \
  "v_cvt_pk_bf16_f32 v20, v20, v21\n" \
  "v_cvt_pk_bf16_f32 v21, v22, v23\n" \
  "v_cvt_pk_bf16_f32 v22, v24, v25\n" \
  "v_cvt_pk_bf16_f32 v23, v26, v27\n" \
  "v_cvt_pk_bf16_f32 v24, v28, v29\n" \
  "v_cvt_pk_bf16_f32 v25, v30, v31\n" \
  "v_cvt_pk_bf16_f32 v26, v32, v33\n" \
  "v_cvt_pk_bf16_f32 v27, v34, v35\n" \
  "v_cvt_pk_bf16_f32 v28, v36, v37\n" \
  "v_cvt_pk_bf16_f32 v29, v38, v39\n" \
  "v_max_f32_e32 v60, v68, v69\n" \
  "v_min_f32_e32 v61, v68, v69\n" \
  "v_max_f32_e32 v62, v70, v71\n" \
  "v_min_f32_e32 v63, v70, v71\n" \
  "v_max3_f32 v60, v60, v72, v73\n" \
  "v_min3_f32 v61, v61, v72, v73\n" \
  "v_max3_f32 v62, v62, v74, v75\n" \
  "v_min3_f32 v63, v63, v74, v75\n" \
  "v_max3_f32 v60, v60, v76, v77\n" \
  "v_min3_f32 v61, v61, v76, v77\n" \
  "v_max3_f32 v62, v62, v78, v79\n" \
  "v_min3_f32 v63, v63, v78, v79\n" \
  "v_max3_f32 v60, v60, v80, v81\n" \
  "v_min3_f32 v61, v61, v80, v81\n" \
  "v_max3_f32 v62, v62, v82, v83\n" \
  "v_min3_f32 v63, v63, v82, v83\n" \
  "v_max3_f32 v60, v60, v84, v85\n" \
  "v_min3_f32 v61, v61, v84, v85\n" \
  "v_max3_f32 v62, v62, v86, v87\n" \
  "v_min3_f32 v63, v63, v86, v87\n" \
  "v_max_f32_e32 v64, v60, v62\n" \
  "v_min_f32_e32 v66, v61, v63\n" \
  "v_mul_f32_e32 v64, s70, v64\n" \
  "v_mul_f32_e32 v66, s70, v66\n" \
  "v_pk_fma_f32 v[40:41], v[72:73], s[70:71], v[66:67] op_sel_hi:[1,0,0] neg_lo:[1,0,0] neg_hi:[1,0,0]\n" \
  "v_pk_fma_f32 v[72:73], v[72:73], s[70:71], v[64:65] op_sel_hi:[1,0,0] neg_lo:[0,0,1] neg_hi:[0,0,1]\n" \
  "v_pk_fma_f32 v[42:43], v[74:75], s[70:71], v[66:67] op_sel_hi:[1,0,0] neg_lo:[1,0,0] neg_hi:[1,0,0]\n" \
  "v_pk_fma_f32 v[74:75], v[74:75], s[70:71], v[64:65] op_sel_hi:[1,0,0] neg_lo:[0,0,1] neg_hi:[0,0,1]\n" \
  "v_pk_fma_f32 v[44:45], v[76:77], s[70:71], v[66:67] op_sel_hi:[1,0,0] neg_lo:[1,0,0] neg_hi:[1,0,0]\n" \
  "v_pk_fma_f32 v[76:77], v[76:77], s[70:71], v[64:65] op_sel_hi:[1,0,0] neg_lo:[0,0,1] neg_hi:[0,0,1]\n" \
  "v_pk_fma_f32 v[46:47], v[78:79], s[70:71], v[66:67] op_sel_hi:[1,0,0] neg_lo:[1,0,0] neg_hi:[1,0,0]\n" \
  "v_pk_fma_f32 v[78:79], v[78:79], s[70:71], v[64:65] op_sel_hi:[1,0,0] neg_lo:[0,0,1] neg_hi:[0,0,1]\n" \
  "v_pk_fma_f32 v[48:49], v[80:81], s[70:71], v[66:67] op_sel_hi:[1,0,0] neg_lo:[1,0,0] neg_hi:[1,0,0]\n" \
  "v_pk_fma_f32 v[80:81], v[80:81], s[70:71], v[64:65] op_sel_hi:[1,0,0] neg_lo:[0,0,1] neg_hi:[0,0,1]\n" \
  "v_pk_fma_f32 v[50:51], v[82:83], s[70:71], v[66:67] op_sel_hi:[1,0,0] neg_lo:[1,0,0] neg_hi:[1,0,0]\n" \
  "v_pk_fma_f32 v[82:83], v[82:83], s[70:71], v[64:65] op_sel_hi:[1,0,0] neg_lo:[0,0,1] neg_hi:[0,0,1]\n" \
  "v_pk_fma_f32 v[52:53], v[84:85], s[70:71], v[66:67] op_sel_hi:[1,0,0] neg_lo:[1,0,0] neg_hi:[1,0,0]\n" \
  "v_pk_fma_f32 v[84:85], v[84:85], s[70:71], v[64:65] op_sel_hi:[1,0,0] neg_lo:[0,0,1] neg_hi:[0,0,1]\n" \
  "v_pk_fma_f32 v[54:55], v[86:87], s[70:71], v[66:67] op_sel_hi:[1,0,0] neg_lo:[1,0,0] neg_hi:[1,0,0]\n" \
  "v_pk_fma_f32 v[86:87], v[86:87], s[70:71], v[64:65] op_sel_hi:[1,0,0] neg_lo:[0,0,1] neg_hi:[0,0,1]\n" \
  "v_pk_fma_f32 v[56:57], v[68:69], s[70:71], v[66:67] op_sel_hi:[1,0,0] neg_lo:[1,0,0] neg_hi:[1,0,0]\n" \
  "v_pk_fma_f32 v[68:69], v[68:69], s[70:71], v[64:65] op_sel_hi:[1,0,0] neg_lo:[0,0,1] neg_hi:[0,0,1]\n" \
  "v_pk_fma_f32 v[58:59], v[70:71], s[70:71], v[66:67] op_sel_hi:[1,0,0] neg_lo:[1,0,0] neg_hi:[1,0,0]\n" \
  "v_pk_fma_f32 v[70:71], v[70:71], s[70:71], v[64:65] op_sel_hi:[1,0,0] neg_lo:[0,0,1] neg_hi:[0,0,1]\n" \
  "v_exp_f32_e32 v72, v72\n" \
  "v_exp_f32_e32 v40, v40\n" \
  "v_exp_f32_e32 v73, v73\n" \
  "v_exp_f32_e32 v41, v41\n" \
  "v_exp_f32_e32 v74, v74\n" \
  "v_exp_f32_e32 v42, v42\n" \
  "v_exp_f32_e32 v75, v75\n" \
  "v_exp_f32_e32 v43, v43\n" \
  "v_exp_f32_e32 v76, v76\n" \
  "v_exp_f32_e32 v44, v44\n" \
  "v_exp_f32_e32 v77, v77\n" \
  "v_exp_f32_e32 v45, v45\n" \
  "v_exp_f32_e32 v78, v78\n" \
  "v_exp_f32_e32 v46, v46\n" \
  "v_exp_f32_e32 v79, v79\n" \
  "v_exp_f32_e32 v47, v47\n" \
  "v_exp_f32_e32 v80, v80\n" \
  "v_exp_f32_e32 v48, v48\n" \
  "v_exp_f32_e32 v81, v81\n" \
  "v_exp_f32_e32 v49, v49\n" \
  "ds_read_b32 v4, v8 offset:136\n" \
  "v_exp_f32_e32 v82, v82\n" \
  "v_exp_f32_e32 v50, v50\n" \
  "v_exp_f32_e32 v83, v83\n" \
  "v_exp_f32_e32 v51, v51\n" \
  "v_exp_f32_e32 v84, v84\n" \
  "v_exp_f32_e32 v52, v52\n" \
  "v_exp_f32_e32 v85, v85\n" \
  "v_exp_f32_e32 v53, v53\n" \
  "v_exp_f32_e32 v86, v86\n" \
  "v_exp_f32_e32 v54, v54\n" \
  "v_exp_f32_e32 v87, v87\n" \
  "v_exp_f32_e32 v55, v55\n" \
  "v_exp_f32_e32 v68, v68\n" \
  "v_exp_f32_e32 v56, v56\n" \
  "v_exp_f32_e32 v69, v69\n" \
  "v_exp_f32_e32 v57, v57\n" \
  "v_exp_f32_e32 v70, v70\n" \
  "v_exp_f32_e32 v58, v58\n" \
  "v_exp_f32_e32 v71, v71\n" \
  "v_exp_f32_e32 v59, v59\n" \
  MID(3) \
  "s_waitcnt lgkmcnt(0)\n" \
  "v_cmp_gt_u32_e32 vcc, 1, v4\n" \
  "s_cbranch_vccnz LO_s\n" \
  "LO_k:\n" \
  MID(4) \
  "ds_read_b128 a[0:3], v88 offset:0\n" \
  "ds_read_b128 a[4:7], v89 offset:0\n" \
  "ds_read_b128 a[8:11], v88 offset:256\n" \
  "ds_read_b128 a[12:15], v89 offset:96\n" \
  "ds_read_b128 a[16:19], v88 offset:512\n" \
  "ds_read_b128 a[20:23], v89 offset:192\n" \
  "ds_read_b128 a[24:27], v88 offset:768\n" \
  "ds_read_b128 a[28:31], v89 offset:288\n" \
  "ds_read_b128 a[32:35], v88 offset:1024\n" \
  "ds_read_b128 a[36:39], v89 offset:384\n" \
  "ds_read_b128 a[40:43], v90 offset:0\n" \
  "ds_read_b128 a[44:47], v91 offset:0\n" \
  "ds_read_b128 a[48:51], v90 offset:256\n" \
  "ds_read_b128 a[52:55], v91 offset:96\n" \
  "v_pk_add_f32 v[64:65], v[72:73], v[74:75]\n" \
  "v_pk_add_f32 v[66:67], v[40:41], v[42:43]\n" \
  "v_pk_add_f32 v[64:65], v[64:65], v[76:77]\n" \
  "v_pk_add_f32 v[66:67], v[66:67], v[44:45]\n" \
  "v_pk_add_f32 v[64:65], v[64:65], v[78:79]\n" \
  "v_pk_add_f32 v[66:67], v[66:67], v[46:47]\n" \
  "v_pk_add_f32 v[64:65], v[64:65], v[80:81]\n" \
  "v_pk_add_f32 v[66:67], v[66:67], v[48:49]\n" \
  "v_pk_add_f32 v[64:65], v[64:65], v[82:83]\n" \
  "v_pk_add_f32 v[66:67], v[66:67], v[50:51]\n" \
  "v_pk_add_f32 v[64:65], v[64:65], v[84:85]\n" \
  "v_pk_add_f32 v[66:67], v[66:67], v[52:53]\n" \
  "v_pk_add_f32 v[64:65], v[64:65], v[86:87]\n" \
  "v_pk_add_f32 v[66:67], v[66:67], v[54:55]\n" \
  "v_pk_add_f32 v[64:65], v[64:65], v[68:69]\n" \
  "v_pk_add_f32 v[66:67], v[66:67], v[56:57]\n" \
  "v_pk_add_f32 v[64:65], v[64:65], v[70:71]\n" \
  "v_pk_add_f32 v[66:67], v[66:67], v[58:59]\n" \
  "v_add_f32_e32 v7, v64, v65\n" \
  "v_add_f32_e32 v66, v66, v67\n" \
  "v_rcp_f32_e32 v66, v66\n" \
  "v_cvt_pk_bf16_f32 v30, v72, v73\n" \
  "v_mul_f32_e32 v66, v66, v7\n" \
  "v_pk_mul_f32 v[40:41], v[40:41], v[66:67] op_sel_hi:[1,0]\n" \
  "v_pk_mul_f32 v[42:43], v[42:43], v[66:67] op_sel_hi:[1,0]\n" \
  "v_pk_mul_f32 v[44:45], v[44:45], v[66:67] op_sel_hi:[1,0]\n" \
  "v_pk_mul_f32 v[46:47], v[46:47], v[66:67] op_sel_hi:[1,0]\n" \
  "v_pk_mul_f32 v[48:49], v[48:49], v[66:67] op_sel_hi:[1,0]\n" \
  "v_pk_mul_f32 v[50:51], v[50:51], v[66:67] op_sel_hi:[1,0]\n" \
  "v_pk_mul_f32 v[52:53], v[52:53], v[66:67] op_sel_hi:[1,0]\n" \
  "v_pk_mul_f32 v[54:55], v[54:55], v[66:67] op_sel_hi:[1,0]\n" \
  "v_pk_mul_f32 v[56:57], v[56:57], v[66:67] op_sel_hi:[1,0]\n" \
  "v_pk_mul_f32 v[58:59], v[58:59], v[66:67] op_sel_hi:[1,0]\n" \
  "s_waitcnt lgkmcnt(13)\n" \
  "v_mfma_f32_32x32x16_bf16 v[96:111], a[0:3], v[10:13], 0\n" \
  "ds_read_b128 a[56:59], v90 offset:512\n" \
  "v_cvt_pk_bf16_f32 v31, v74, v75\n" \
  "v_cvt_pk_bf16_f32 v32, v76, v77\n" \
  "v_cvt_pk_bf16_f32 v33, v78, v79\n" \
  "s_waitcnt lgkmcnt(13)\n" \
  "v_mfma_f32_32x32x16_bf16 v[112:127], a[4:7], v[10:13], 0\n" \
  "ds_read_b128 a[60:63], v91 offset:192\n" \
  "v_cvt_pk_bf16_f32 v34, v80, v81\n" \
  "v_cvt_pk_bf16_f32 v35, v82, v83\n" \
  "v_cvt_pk_bf16_f32 v36, v84, v85\n" \
  "s_waitcnt lgkmcnt(13)\n" \
  "v_mfma_f32_32x32x16_bf16 v[96:111], a[8:11], v[14:17], v[96:111]\n" \
  "ds_read_b128 a[64:67], v90 offset:768\n" \
  "v_cvt_pk_bf16_f32 v37, v86, v87\n" \
  "v_cvt_pk_bf16_f32 v38, v68, v69\n" \
  "v_cvt_pk_bf16_f32 v39, v70, v71\n" \
  "s_waitcnt lgkmcnt(13)\n" \
  "v_mfma_f32_32x32x16_bf16 v[112:127], a[12:15], v[14:17], v[112:127]\n" \
  "ds_read_b128 a[68:71], v91 offset:288\n" \
  "v_cvt_pk_bf16_f32 v40, v40, v41\n" \
  "v_cvt_pk_bf16_f32 v41, v42, v43\n" \
  "v_cvt_pk_bf16_f32 v42, v44, v45\n" \
  "s_waitcnt lgkmcnt(13)\n" \
  "v_mfma_f32_32x32x16_bf16 v[96:111], a[16:19], v[18:21], v[96:111]\n" \
  "ds_read_b128 a[72:75], v90 offset:1024\n" \
  "v_cvt_pk_bf16_f32 v43, v46, v47\n" \
  "v_cvt_pk_bf16_f32 v44, v48, v49\n" \
  "v_cvt_pk_bf16_f32 v45, v50, v51\n" \
  "s_waitcnt lgkmcnt(13)\n" \
  "v_mfma_f32_32x32x16_bf16 v[112:127], a[20:23], v[18:21], v[112:127]\n" \
  "ds_read_b128 a[76:79], v91 offset:384\n" \
  "v_cvt_pk_bf16_f32 v46, v52, v53\n" \
  "v_cvt_pk_bf16_f32 v47, v54, v55\n" \
  "v_cvt_pk_bf16_f32 v48, v56, v57\n" \
  "s_waitcnt lgkmcnt(13)\n" \
  "v_mfma_f32_32x32x16_bf16 v[96:111], a[24:27], v[22:25], v[96:111]\n" \
  "v_cvt_pk_bf16_f32 v49, v58, v59\n" \
  "s_waitcnt lgkmcnt(12)\n" \
  "v_mfma_f32_32x32x16_bf16 v[112:127], a[28:31], v[22:25], v[112:127]\n" \
  "s_waitcnt lgkmcnt(11)\n" \
  "v_mfma_f32_32x32x16_bf16 v[96:111], a[32:35], v[26:29], v[96:111]\n" \
  "s_waitcnt lgkmcnt(10)\n" \
  "v_mfma_f32_32x32x16_bf16 v[112:127], a[36:39], v[26:29], v[112:127]\n" \
  "s_waitcnt lgkmcnt(9)\n" \
  "v_mfma_f32_32x32x16_bf16 v[50:65], a[40:43], v[30:33], 0\n" \
  "s_waitcnt lgkmcnt(8)\n" \
  "v_mfma_f32_32x32x16_bf16 v[66:81], a[44:47], v[30:33], 0\n" \
  "s_nop 7\n" \
  "s_waitcnt lgkmcnt(7)\n" \
  "v_mfma_f32_32x32x16_bf16 v[50:65], a[48:51], v[34:37], v[50:65]\n" \
  "v_fmac_f32_e32 v116, 0x358637bd, v6\n" \
  "v_rcp_f32_e32 v116, v116\n" \
  "s_nop 0\n" \
  "v_mul_f32_e32 v96, v96, v116\n" \
  "v_mul_f32_e32 v97, v97, v116\n" \
  "s_waitcnt lgkmcnt(6)\n" \
  "v_mfma_f32_32x32x16_bf16 v[66:81], a[52:55], v[34:37], v[66:81]\n" \
  "v_mul_f32_e32 v98, v98, v116\n" \
  "v_mul_f32_e32 v99, v99, v116\n" \
  "v_mul_f32_e32 v100, v100, v116\n" \
  "v_mul_f32_e32 v101, v101, v116\n" \
  "v_mul_f32_e32 v102, v102, v116\n" \
  "s_waitcnt lgkmcnt(5)\n" \
  "v_mfma_f32_32x32x16_bf16 v[50:65], a[56:59], v[38:41], v[50:65]\n" \
  "v_mul_f32_e32 v103, v103, v116\n" \
  "v_mul_f32_e32 v104, v104, v116\n" \
  "v_mul_f32_e32 v105, v105, v116\n" \
  "v_mul_f32_e32 v106, v106, v116\n" \
  "v_mul_f32_e32 v107, v107, v116\n" \
  "s_waitcnt lgkmcnt(4)\n" \
  "v_mfma_f32_32x32x16_bf16 v[66:81], a[60:63], v[38:41], v[66:81]\n" \
  "v_mul_f32_e32 v108, v108, v116\n" \
  "v_mul_f32_e32 v109, v109, v116\n" \
  "v_mul_f32_e32 v110, v110, v116\n" \
  "v_mul_f32_e32 v111, v111, v116\n" \
  "v_mul_f32_e32 v112, v112, v116\n" \
  "s_waitcnt lgkmcnt(3)\n" \
  "v_mfma_f32_32x32x16_bf16 v[50:65], a[64:67], v[42:45], v[50:65]\n" \
  "v_mul_f32_e32 v113, v113, v116\n" \
  "v_mul_f32_e32 v114, v114, v116\n" \
  "v_mul_f32_e32 v115, v115, v116\n" \
  "v_cvt_pk_bf16_f32 v10, v96, v97\n" \
  "v_cvt_pk_bf16_f32 v11, v98, v99\n" \
  "s_waitcnt lgkmcnt(2)\n" \
  "v_mfma_f32_32x32x16_bf16 v[66:81], a[68:71], v[42:45], v[66:81]\n" \
  "v_cvt_pk_bf16_f32 v12, v100, v101\n" \
  "v_cvt_pk_bf16_f32 v13, v102, v103\n" \
  "global_store_dwordx4 v92, v[10:13], s[68:69] sc1\n" \
  "v_cvt_pk_bf16_f32 v14, v104, v105\n" \
  "v_cvt_pk_bf16_f32 v15, v106, v107\n" \
  "s_waitcnt lgkmcnt(1)\n" \
  "v_mfma_f32_32x32x16_bf16 v[50:65], a[72:75], v[46:49], v[50:65]\n" \
  "v_cvt_pk_bf16_f32 v16, v108, v109\n" \
  "v_cvt_pk_bf16_f32 v17, v110, v111\n" \
  "global_store_dwordx4 v93, v[14:17], s[68:69] sc1\n" \
  "v_cvt_pk_bf16_f32 v18, v112, v113\n" \
  "v_cvt_pk_bf16_f32 v19, v114, v115\n" \
  "s_waitcnt lgkmcnt(0)\n" \
  "v_mfma_f32_32x32x16_bf16 v[66:81], a[76:79], v[46:49], v[66:81]\n" \
  "s_nop 11\n" \
  "v_fmac_f32_e32 v70, 0x358637bd, v7\n" \
  "v_rcp_f32_e32 v70, v70\n" \
  "s_nop 0\n" \
  "v_pk_mul_f32 v[50:51], v[50:51], v[70:71] op_sel_hi:[1,0]\n" \
  "v_pk_mul_f32 v[52:53], v[52:53], v[70:71] op_sel_hi:[1,0]\n" \
  "v_pk_mul_f32 v[54:55], v[54:55], v[70:71] op_sel_hi:[1,0]\n" \
  "v_pk_mul_f32 v[56:57], v[56:57], v[70:71] op_sel_hi:[1,0]\n" \
  "v_pk_mul_f32 v[58:59], v[58:59], v[70:71] op_sel_hi:[1,0]\n" \
  "v_pk_mul_f32 v[60:61], v[60:61], v[70:71] op_sel_hi:[1,0]\n" \
  "v_pk_mul_f32 v[62:63], v[62:63], v[70:71] op_sel_hi:[1,0]\n" \
  "v_pk_mul_f32 v[64:65], v[64:65], v[70:71] op_sel_hi:[1,0]\n" \
  "v_pk_mul_f32 v[66:67], v[66:67], v[70:71] op_sel_hi:[1,0]\n" \
  "v_pk_mul_f32 v[68:69], v[68:69], v[70:71] op_sel_hi:[1,0]\n" \
  "v_cvt_pk_bf16_f32 v20, v50, v51\n" \
  "v_cvt_pk_bf16_f32 v21, v52, v53\n" \
  "global_store_dwordx4 v94, v[18:21], s[68:69] sc1\n" \
  "v_cvt_pk_bf16_f32 v22, v54, v55\n" \
  "v_cvt_pk_bf16_f32 v23, v56, v57\n" \
  "v_cvt_pk_bf16_f32 v24, v58, v59\n" \
  "v_cvt_pk_bf16_f32 v25, v60, v61\n" \
  "global_store_dwordx4 v95, v[22:25], s[68:69] sc1\n" \
  "v_cvt_pk_bf16_f32 v26, v62, v63\n" \
  "v_cvt_pk_bf16_f32 v27, v64, v65\n" \
  "v_cvt_pk_bf16_f32 v28, v66, v67\n" \
  "v_cvt_pk_bf16_f32 v29, v68, v69\n" \
  "global_store_dwordx4 v5, v[26:29], s[68:69] sc1\n" \
  "ds_add_u32 v3, v2 offset:16\n" \
  "LEND:\n" \
  ""
#define P_BODY \
  "global_load_dwordx4 a[0:3], v6, s[44:45] offset:0 sc1\n" \
  "global_load_dwordx4 a[4:7], v6, s[44:45] offset:1024 sc1\n" \
  "global_load_dwordx4 a[8:11], v7, s[44:45] offset:0 sc1\n" \
  "global_load_dwordx4 a[12:15], v7, s[44:45] offset:1024 sc1\n" \
  "global_load_dwordx4 a[16:19], v6, s[46:47] offset:0 sc1\n" \
  "global_load_dwordx4 a[20:23], v6, s[46:47] offset:1024 sc1\n" \
  "global_load_dwordx4 a[24:27], v7, s[46:47] offset:0 sc1\n" \
  "global_load_dwordx4 a[28:31], v7, s[46:47] offset:1024 sc1\n" \
  "global_load_dwordx4 a[32:35], v6, s[48:49] offset:0 sc1\n" \
  "global_load_dwordx4 a[36:39], v6, s[48:49] offset:1024 sc1\n" \
  "global_load_dwordx4 a[40:43], v7, s[48:49] offset:0 sc1\n" \
  "global_load_dwordx4 a[44:47], v7, s[48:49] offset:1024 sc1\n" \
  "v_mov_b32_e32 v12, 81984\n" \
  "s_mov_b32 s75, 0x40000\n" \
  "LPW_s:\n" \
  "ds_read_b32 v13, v12\n" \
  "s_sub_u32 s75, s75, 1\n" \
  "s_cbranch_scc1 LPW_k\n" \
  "s_waitcnt lgkmcnt(0)\n" \
  "v_cmp_gt_u32_e32 vcc, 4, v13\n" \
  "s_cbranch_vccz LPW_k\n" \
  "s_sleep 1\n" \
  "s_branch LPW_s\n" \
  "LPW_k:\n" \
  "v_mov_b32_e32 v38, s66\n" \
  "v_mov_b32_e32 v39, s67\n" \
  "ds_read_b64_tr_b16 v[12:13], v4 offset:0\n" \
  "ds_read_b64_tr_b16 v[14:15], v4 offset:1280\n" \
  "ds_read_b64_tr_b16 v[16:17], v4 offset:8\n" \
  "ds_read_b64_tr_b16 v[18:19], v4 offset:1288\n" \
  "ds_read_b64_tr_b16 v[20:21], v4 offset:16\n" \
  "ds_read_b64_tr_b16 v[22:23], v4 offset:1296\n" \
  "ds_read_b64_tr_b16 v[24:25], v4 offset:24\n" \
  "ds_read_b64_tr_b16 v[26:27], v4 offset:1304\n" \
  "s_waitcnt lgkmcnt(6) vmcnt(10)\n" \
  "v_mfma_f32_16x16x32_bf16 v[48:51], v[12:15], a[0:3], 0\n" \
  "ds_read_b64_tr_b16 v[28:29], v4 offset:32\n" \
  "s_or_b64 exec, s[68:69], 1\n" \
  "global_load_dwordx2 v[38:39], v8, s[64:65] sc1\n" \
  "s_mov_b64 exec, -1\n" \
  "v_mfma_f32_16x16x32_bf16 v[68:71], v[12:15], a[4:7], 0\n" \
  "ds_read_b64_tr_b16 v[30:31], v4 offset:1312\n" \
  "s_waitcnt lgkmcnt(6)\n" \
  "v_mfma_f32_16x16x32_bf16 v[52:55], v[16:19], a[0:3], 0\n" \
  "ds_read_b64_tr_b16 v[12:13], v4 offset:5120\n" \
  "global_load_dwordx4 a[48:51], v6, s[50:51] offset:0 sc1\n" \
  "v_mfma_f32_16x16x32_bf16 v[72:75], v[16:19], a[4:7], 0\n" \
  "ds_read_b64_tr_b16 v[14:15], v4 offset:6400\n" \
  "s_waitcnt lgkmcnt(6)\n" \
  "v_mfma_f32_16x16x32_bf16 v[56:59], v[20:23], a[0:3], 0\n" \
  "ds_read_b64_tr_b16 v[16:17], v4 offset:5128\n" \
  "v_mfma_f32_16x16x32_bf16 v[76:79], v[20:23], a[4:7], 0\n" \
  "ds_read_b64_tr_b16 v[18:19], v4 offset:6408\n" \
  "s_waitcnt lgkmcnt(6)\n" \
  "v_mfma_f32_16x16x32_bf16 v[60:63], v[24:27], a[0:3], 0\n" \
  "ds_read_b64_tr_b16 v[20:21], v4 offset:5136\n" \
  "global_load_dwordx4 a[52:55], v6, s[50:51] offset:1024 sc1\n" \
  "v_mfma_f32_16x16x32_bf16 v[80:83], v[24:27], a[4:7], 0\n" \
  "ds_read_b64_tr_b16 v[22:23], v4 offset:6416\n" \
  "s_waitcnt lgkmcnt(6)\n" \
  "v_mfma_f32_16x16x32_bf16 v[64:67], v[28:31], a[0:3], 0\n" \
  "ds_read_b64_tr_b16 v[24:25], v4 offset:5144\n" \
  "v_mfma_f32_16x16x32_bf16 v[84:87], v[28:31], a[4:7], 0\n" \
  "ds_read_b64_tr_b16 v[26:27], v4 offset:6424\n" \
  "s_waitcnt lgkmcnt(6) vmcnt(11)\n" \
  "v_mfma_f32_16x16x32_bf16 v[48:51], v[12:15], a[8:11], v[48:51]\n" \
  "ds_read_b64_tr_b16 v[28:29], v4 offset:5152\n" \
  "global_load_dwordx4 a[56:59], v7, s[50:51] offset:0 sc1\n" \
  "v_mfma_f32_16x16x32_bf16 v[68:71], v[12:15], a[12:15], v[68:71]\n" \
  "ds_read_b64_tr_b16 v[30:31], v4 offset:6432\n" \
  "s_waitcnt lgkmcnt(6)\n" \
  "v_mfma_f32_16x16x32_bf16 v[52:55], v[16:19], a[8:11], v[52:55]\n" \
  "ds_read_b64_tr_b16 v[12:13], v4 offset:10240\n" \
  "v_mfma_f32_16x16x32_bf16 v[72:75], v[16:19], a[12:15], v[72:75]\n" \
  "ds_read_b64_tr_b16 v[14:15], v4 offset:11520\n" \
  "s_waitcnt lgkmcnt(6)\n" \
  "v_mfma_f32_16x16x32_bf16 v[56:59], v[20:23], a[8:11], v[56:59]\n" \
  "ds_read_b64_tr_b16 v[16:17], v4 offset:10248\n" \
  "global_load_dwordx4 a[60:63], v7, s[50:51] offset:1024 sc1\n" \
  "v_mfma_f32_16x16x32_bf16 v[76:79], v[20:23], a[12:15], v[76:79]\n" \
  "ds_read_b64_tr_b16 v[18:19], v4 offset:11528\n" \
  "s_waitcnt lgkmcnt(6)\n" \
  "v_mfma_f32_16x16x32_bf16 v[60:63], v[24:27], a[8:11], v[60:63]\n" \
  "ds_read_b64_tr_b16 v[20:21], v4 offset:10256\n" \
  "v_mfma_f32_16x16x32_bf16 v[80:83], v[24:27], a[12:15], v[80:83]\n" \
  "ds_read_b64_tr_b16 v[22:23], v4 offset:11536\n" \
  "s_waitcnt lgkmcnt(6)\n" \
  "v_mfma_f32_16x16x32_bf16 v[64:67], v[28:31], a[8:11], v[64:67]\n" \
  "ds_read_b64_tr_b16 v[24:25], v4 offset:10264\n" \
  "v_mfma_f32_16x16x32_bf16 v[84:87], v[28:31], a[12:15], v[84:87]\n" \
  "ds_read_b64_tr_b16 v[26:27], v4 offset:11544\n" \
  "s_waitcnt lgkmcnt(6) vmcnt(11)\n" \
  "v_mfma_f32_16x16x32_bf16 v[48:51], v[12:15], a[16:19], v[48:51]\n" \
  "ds_read_b64_tr_b16 v[28:29], v4 offset:10272\n" \
  "v_mfma_f32_16x16x32_bf16 v[68:71], v[12:15], a[20:23], v[68:71]\n" \
  "ds_read_b64_tr_b16 v[30:31], v4 offset:11552\n" \
  "s_waitcnt lgkmcnt(6)\n" \
  "v_mfma_f32_16x16x32_bf16 v[52:55], v[16:19], a[16:19], v[52:55]\n" \
  "ds_read_b64_tr_b16 v[12:13], v4 offset:15360\n" \
  "global_load_dwordx4 a[64:67], v6, s[52:53] offset:0 sc1\n" \
  "v_mfma_f32_16x16x32_bf16 v[72:75], v[16:19], a[20:23], v[72:75]\n" \
  "ds_read_b64_tr_b16 v[14:15], v4 offset:16640\n" \
  "s_waitcnt lgkmcnt(6)\n" \
  "v_mfma_f32_16x16x32_bf16 v[56:59], v[20:23], a[16:19], v[56:59]\n" \
  "ds_read_b64_tr_b16 v[16:17], v4 offset:15368\n" \
  "v_mfma_f32_16x16x32_bf16 v[76:79], v[20:23], a[20:23], v[76:79]\n" \
  "ds_read_b64_tr_b16 v[18:19], v4 offset:16648\n" \
  "s_waitcnt lgkmcnt(6)\n" \
  "v_mfma_f32_16x16x32_bf16 v[60:63], v[24:27], a[16:19], v[60:63]\n" \
  "ds_read_b64_tr_b16 v[20:21], v4 offset:15376\n" \
  "global_load_dwordx4 a[68:71], v6, s[52:53] offset:1024 sc1\n" \
  "v_mfma_f32_16x16x32_bf16 v[80:83], v[24:27], a[20:23], v[80:83]\n" \
  "ds_read_b64_tr_b16 v[22:23], v4 offset:16656\n" \
  "s_waitcnt lgkmcnt(6)\n" \
  "v_mfma_f32_16x16x32_bf16 v[64:67], v[28:31], a[16:19], v[64:67]\n" \
  "ds_read_b64_tr_b16 v[24:25], v4 offset:15384\n" \
  "v_mfma_f32_16x16x32_bf16 v[84:87], v[28:31], a[20:23], v[84:87]\n" \
  "ds_read_b64_tr_b16 v[26:27], v4 offset:16664\n" \
  "s_waitcnt lgkmcnt(6) vmcnt(11)\n" \
  "v_mfma_f32_16x16x32_bf16 v[48:51], v[12:15], a[24:27], v[48:51]\n" \
  "ds_read_b64_tr_b16 v[28:29], v4 offset:15392\n" \
  "global_load_dwordx4 a[72:75], v7, s[52:53] offset:0 sc1\n" \
  "v_mfma_f32_16x16x32_bf16 v[68:71], v[12:15], a[28:31], v[68:71]\n" \
  "ds_read_b64_tr_b16 v[30:31], v4 offset:16672\n" \
  "s_waitcnt lgkmcnt(6)\n" \
  "v_mfma_f32_16x16x32_bf16 v[52:55], v[16:19], a[24:27], v[52:55]\n" \
  "ds_read_b64_tr_b16 v[12:13], v4 offset:20480\n" \
  "v_mfma_f32_16x16x32_bf16 v[72:75], v[16:19], a[28:31], v[72:75]\n" \
  "ds_read_b64_tr_b16 v[14:15], v4 offset:21760\n" \
  "s_waitcnt lgkmcnt(6)\n" \
  "v_mfma_f32_16x16x32_bf16 v[56:59], v[20:23], a[24:27], v[56:59]\n" \
  "ds_read_b64_tr_b16 v[16:17], v4 offset:20488\n" \
  "global_load_dwordx4 a[76:79], v7, s[52:53] offset:1024 sc1\n" \
  "v_mfma_f32_16x16x32_bf16 v[76:79], v[20:23], a[28:31], v[76:79]\n" \
  "ds_read_b64_tr_b16 v[18:19], v4 offset:21768\n" \
  "s_waitcnt lgkmcnt(6)\n" \
  "v_mfma_f32_16x16x32_bf16 v[60:63], v[24:27], a[24:27], v[60:63]\n" \
  "ds_read_b64_tr_b16 v[20:21], v4 offset:20496\n" \
  "v_mfma_f32_16x16x32_bf16 v[80:83], v[24:27], a[28:31], v[80:83]\n" \
  "ds_read_b64_tr_b16 v[22:23], v4 offset:21776\n" \
  "s_waitcnt lgkmcnt(6)\n" \
  "v_mfma_f32_16x16x32_bf16 v[64:67], v[28:31], a[24:27], v[64:67]\n" \
  "ds_read_b64_tr_b16 v[24:25], v4 offset:20504\n" \
  "v_mfma_f32_16x16x32_bf16 v[84:87], v[28:31], a[28:31], v[84:87]\n" \
  "ds_read_b64_tr_b16 v[26:27], v4 offset:21784\n" \
  "s_waitcnt lgkmcnt(6) vmcnt(8)\n" \
  "v_mfma_f32_16x16x32_bf16 v[48:51], v[12:15], a[32:35], v[48:51]\n" \
  "ds_read_b64_tr_b16 v[28:29], v4 offset:20512\n" \
  "v_cmp_ne_u64_e32 vcc, s[66:67], v[38:39]\n" \
  "s_cbranch_vccnz LTG_s\n" \
  "LTG_k:\n" \
  "v_mfma_f32_16x16x32_bf16 v[68:71], v[12:15], a[36:39], v[68:71]\n" \
  "ds_read_b64_tr_b16 v[30:31], v4 offset:21792\n" \
  "s_waitcnt lgkmcnt(6)\n" \
  "v_mfma_f32_16x16x32_bf16 v[52:55], v[16:19], a[32:35], v[52:55]\n" \
  "ds_read_b64_tr_b16 v[12:13], v4 offset:25600\n" \
  "global_load_dwordx4 a[80:83], v6, s[54:55] offset:0 sc1\n" \
  "v_mfma_f32_16x16x32_bf16 v[72:75], v[16:19], a[36:39], v[72:75]\n" \
  "ds_read_b64_tr_b16 v[14:15], v4 offset:26880\n" \
  "s_waitcnt lgkmcnt(6)\n" \
  "v_mfma_f32_16x16x32_bf16 v[56:59], v[20:23], a[32:35], v[56:59]\n" \
  "ds_read_b64_tr_b16 v[16:17], v4 offset:25608\n" \
  "v_mfma_f32_16x16x32_bf16 v[76:79], v[20:23], a[36:39], v[76:79]\n" \
  "ds_read_b64_tr_b16 v[18:19], v4 offset:26888\n" \
  "s_waitcnt lgkmcnt(6)\n" \
  "v_mfma_f32_16x16x32_bf16 v[60:63], v[24:27], a[32:35], v[60:63]\n" \
  "ds_read_b64_tr_b16 v[20:21], v4 offset:25616\n" \
  "global_load_dwordx4 a[84:87], v6, s[54:55] offset:1024 sc1\n" \
  "v_mfma_f32_16x16x32_bf16 v[80:83], v[24:27], a[36:39], v[80:83]\n" \
  "ds_read_b64_tr_b16 v[22:23], v4 offset:26896\n" \
  "s_waitcnt lgkmcnt(6)\n" \
  "v_mfma_f32_16x16x32_bf16 v[64:67], v[28:31], a[32:35], v[64:67]\n" \
  "ds_read_b64_tr_b16 v[24:25], v4 offset:25624\n" \
  "v_mfma_f32_16x16x32_bf16 v[84:87], v[28:31], a[36:39], v[84:87]\n" \
  "ds_read_b64_tr_b16 v[26:27], v4 offset:26904\n" \
  "s_waitcnt lgkmcnt(6) vmcnt(11)\n" \
  "v_mfma_f32_16x16x32_bf16 v[48:51], v[12:15], a[40:43], v[48:51]\n" \
  "ds_read_b64_tr_b16 v[28:29], v4 offset:25632\n" \
  "global_load_dwordx4 a[88:91], v7, s[54:55] offset:0 sc1\n" \
  "v_mfma_f32_16x16x32_bf16 v[68:71], v[12:15], a[44:47], v[68:71]\n" \
  "ds_read_b64_tr_b16 v[30:31], v4 offset:26912\n" \
  "s_waitcnt lgkmcnt(6)\n" \
  "v_mfma_f32_16x16x32_bf16 v[52:55], v[16:19], a[40:43], v[52:55]\n" \
  "ds_read_b64_tr_b16 v[12:13], v4 offset:30720\n" \
  "v_mfma_f32_16x16x32_bf16 v[72:75], v[16:19], a[44:47], v[72:75]\n" \
  "ds_read_b64_tr_b16 v[14:15], v4 offset:32000\n" \
  "s_waitcnt lgkmcnt(6)\n" \
  "v_mfma_f32_16x16x32_bf16 v[56:59], v[20:23], a[40:43], v[56:59]\n" \
  "ds_read_b64_tr_b16 v[16:17], v4 offset:30728\n" \
  "global_load_dwordx4 a[92:95], v7, s[54:55] offset:1024 sc1\n" \
  "v_mfma_f32_16x16x32_bf16 v[76:79], v[20:23], a[44:47], v[76:79]\n" \
  "ds_read_b64_tr_b16 v[18:19], v4 offset:32008\n" \
  "s_waitcnt lgkmcnt(6)\n" \
  "v_mfma_f32_16x16x32_bf16 v[60:63], v[24:27], a[40:43], v[60:63]\n" \
  "ds_read_b64_tr_b16 v[20:21], v4 offset:30736\n" \
  "v_mfma_f32_16x16x32_bf16 v[80:83], v[24:27], a[44:47], v[80:83]\n" \
  "ds_read_b64_tr_b16 v[22:23], v4 offset:32016\n" \
  "s_waitcnt lgkmcnt(6)\n" \
  "v_mfma_f32_16x16x32_bf16 v[64:67], v[28:31], a[40:43], v[64:67]\n" \
  "ds_read_b64_tr_b16 v[24:25], v4 offset:30744\n" \
  "v_mfma_f32_16x16x32_bf16 v[84:87], v[28:31], a[44:47], v[84:87]\n" \
  "ds_read_b64_tr_b16 v[26:27], v4 offset:32024\n" \
  "s_waitcnt lgkmcnt(6) vmcnt(10)\n" \
  "v_mfma_f32_16x16x32_bf16 v[48:51], v[12:15], a[48:51], v[48:51]\n" \
  "ds_read_b64_tr_b16 v[28:29], v4 offset:30752\n" \
  "v_mfma_f32_16x16x32_bf16 v[68:71], v[12:15], a[52:55], v[68:71]\n" \
  "ds_read_b64_tr_b16 v[30:31], v4 offset:32032\n" \
  "s_waitcnt lgkmcnt(6)\n" \
  "v_mfma_f32_16x16x32_bf16 v[52:55], v[16:19], a[48:51], v[52:55]\n" \
  "ds_read_b64_tr_b16 v[12:13], v4 offset:35840\n" \
  "global_load_dwordx4 a[96:99], v6, s[56:57] offset:0 sc1\n" \
  "v_mfma_f32_16x16x32_bf16 v[72:75], v[16:19], a[52:55], v[72:75]\n" \
  "ds_read_b64_tr_b16 v[14:15], v4 offset:37120\n" \
  "s_waitcnt lgkmcnt(6)\n" \
  "v_mfma_f32_16x16x32_bf16 v[56:59], v[20:23], a[48:51], v[56:59]\n" \
  "ds_read_b64_tr_b16 v[16:17], v4 offset:35848\n" \
  "v_mfma_f32_16x16x32_bf16 v[76:79], v[20:23], a[52:55], v[76:79]\n" \
  "ds_read_b64_tr_b16 v[18:19], v4 offset:37128\n" \
  "s_waitcnt lgkmcnt(6)\n" \
  "v_mfma_f32_16x16x32_bf16 v[60:63], v[24:27], a[48:51], v[60:63]\n" \
  "ds_read_b64_tr_b16 v[20:21], v4 offset:35856\n" \
  "global_load_dwordx4 a[100:103], v6, s[56:57] offset:1024 sc1\n" \
  "v_mfma_f32_16x16x32_bf16 v[80:83], v[24:27], a[52:55], v[80:83]\n" \
  "ds_read_b64_tr_b16 v[22:23], v4 offset:37136\n" \
  "s_waitcnt lgkmcnt(6)\n" \
  "v_mfma_f32_16x16x32_bf16 v[64:67], v[28:31], a[48:51], v[64:67]\n" \
  "ds_read_b64_tr_b16 v[24:25], v4 offset:35864\n" \
  "v_mfma_f32_16x16x32_bf16 v[84:87], v[28:31], a[52:55], v[84:87]\n" \
  "ds_read_b64_tr_b16 v[26:27], v4 offset:37144\n" \
  "s_waitcnt lgkmcnt(6) vmcnt(10)\n" \
  "v_mfma_f32_16x16x32_bf16 v[48:51], v[12:15], a[56:59], v[48:51]\n" \
  "ds_read_b64_tr_b16 v[28:29], v4 offset:35872\n" \
  "global_load_dwordx4 a[104:107], v7, s[56:57] offset:0 sc1\n" \
  "v_mfma_f32_16x16x32_bf16 v[68:71], v[12:15], a[60:63], v[68:71]\n" \
  "ds_read_b64_tr_b16 v[30:31], v4 offset:37152\n" \
  "s_waitcnt lgkmcnt(6)\n" \
  "v_mfma_f32_16x16x32_bf16 v[52:55], v[16:19], a[56:59], v[52:55]\n" \
  "ds_read_b64_tr_b16 v[12:13], v4 offset:40960\n" \
  "v_mfma_f32_16x16x32_bf16 v[72:75], v[16:19], a[60:63], v[72:75]\n" \
  "ds_read_b64_tr_b16 v[14:15], v4 offset:42240\n" \
  "s_waitcnt lgkmcnt(6)\n" \
  "v_mfma_f32_16x16x32_bf16 v[56:59], v[20:23], a[56:59], v[56:59]\n" \
  "ds_read_b64_tr_b16 v[16:17], v4 offset:40968\n" \
  "global_load_dwordx4 a[108:111], v7, s[56:57] offset:1024 sc1\n" \
  "v_mfma_f32_16x16x32_bf16 v[76:79], v[20:23], a[60:63], v[76:79]\n" \
  "ds_read_b64_tr_b16 v[18:19], v4 offset:42248\n" \
  "s_waitcnt lgkmcnt(6)\n" \
  "v_mfma_f32_16x16x32_bf16 v[60:63], v[24:27], a[56:59], v[60:63]\n" \
  "ds_read_b64_tr_b16 v[20:21], v4 offset:40976\n" \
  "v_mfma_f32_16x16x32_bf16 v[80:83], v[24:27], a[60:63], v[80:83]\n" \
  "ds_read_b64_tr_b16 v[22:23], v4 offset:42256\n" \
  "s_waitcnt lgkmcnt(6)\n" \
  "v_mfma_f32_16x16x32_bf16 v[64:67], v[28:31], a[56:59], v[64:67]\n" \
  "ds_read_b64_tr_b16 v[24:25], v4 offset:40984\n" \
  "v_mfma_f32_16x16x32_bf16 v[84:87], v[28:31], a[60:63], v[84:87]\n" \
  "ds_read_b64_tr_b16 v[26:27], v4 offset:42264\n" \
  "s_waitcnt lgkmcnt(6) vmcnt(10)\n" \
  "v_mfma_f32_16x16x32_bf16 v[48:51], v[12:15], a[64:67], v[48:51]\n" \
  "ds_read_b64_tr_b16 v[28:29], v4 offset:40992\n" \
  "v_mfma_f32_16x16x32_bf16 v[68:71], v[12:15], a[68:71], v[68:71]\n" \
  "ds_read_b64_tr_b16 v[30:31], v4 offset:42272\n" \
  "s_waitcnt lgkmcnt(6)\n" \
  "v_mfma_f32_16x16x32_bf16 v[52:55], v[16:19], a[64:67], v[52:55]\n" \
  "ds_read_b64_tr_b16 v[12:13], v4 offset:46080\n" \
  "global_load_dwordx4 a[112:115], v6, s[58:59] offset:0 sc1\n" \
  "v_mfma_f32_16x16x32_bf16 v[72:75], v[16:19], a[68:71], v[72:75]\n" \
  "ds_read_b64_tr_b16 v[14:15], v4 offset:47360\n" \
  "s_waitcnt lgkmcnt(6)\n" \
  "v_mfma_f32_16x16x32_bf16 v[56:59], v[20:23], a[64:67], v[56:59]\n" \
  "ds_read_b64_tr_b16 v[16:17], v4 offset:46088\n" \
  "v_mfma_f32_16x16x32_bf16 v[76:79], v[20:23], a[68:71], v[76:79]\n" \
  "ds_read_b64_tr_b16 v[18:19], v4 offset:47368\n" \
  "s_waitcnt lgkmcnt(6)\n" \
  "v_mfma_f32_16x16x32_bf16 v[60:63], v[24:27], a[64:67], v[60:63]\n" \
  "ds_read_b64_tr_b16 v[20:21], v4 offset:46096\n" \
  "global_load_dwordx4 a[116:119], v6, s[58:59] offset:1024 sc1\n" \
  "v_mfma_f32_16x16x32_bf16 v[80:83], v[24:27], a[68:71], v[80:83]\n" \
  "ds_read_b64_tr_b16 v[22:23], v4 offset:47376\n" \
  "s_waitcnt lgkmcnt(6)\n" \
  "v_mfma_f32_16x16x32_bf16 v[64:67], v[28:31], a[64:67], v[64:67]\n" \
  "ds_read_b64_tr_b16 v[24:25], v4 offset:46104\n" \
  "v_mfma_f32_16x16x32_bf16 v[84:87], v[28:31], a[68:71], v[84:87]\n" \
  "ds_read_b64_tr_b16 v[26:27], v4 offset:47384\n" \
  "s_waitcnt lgkmcnt(6) vmcnt(10)\n" \
  "v_mfma_f32_16x16x32_bf16 v[48:51], v[12:15], a[72:75], v[48:51]\n" \
  "ds_read_b64_tr_b16 v[28:29], v4 offset:46112\n" \
  "global_load_dwordx4 a[120:123], v7, s[58:59] offset:0 sc1\n" \
  "v_mfma_f32_16x16x32_bf16 v[68:71], v[12:15], a[76:79], v[68:71]\n" \
  "ds_read_b64_tr_b16 v[30:31], v4 offset:47392\n" \
  "s_waitcnt lgkmcnt(6)\n" \
  "v_mfma_f32_16x16x32_bf16 v[52:55], v[16:19], a[72:75], v[52:55]\n" \
  "ds_read_b64_tr_b16 v[12:13], v4 offset:51200\n" \
  "v_mfma_f32_16x16x32_bf16 v[72:75], v[16:19], a[76:79], v[72:75]\n" \
  "ds_read_b64_tr_b16 v[14:15], v4 offset:52480\n" \
  "s_waitcnt lgkmcnt(6)\n" \
  "v_mfma_f32_16x16x32_bf16 v[56:59], v[20:23], a[72:75], v[56:59]\n" \
  "ds_read_b64_tr_b16 v[16:17], v4 offset:51208\n" \
  "global_load_dwordx4 a[124:127], v7, s[58:59] offset:1024 sc1\n" \
  "v_mfma_f32_16x16x32_bf16 v[76:79], v[20:23], a[76:79], v[76:79]\n" \
  "ds_read_b64_tr_b16 v[18:19], v4 offset:52488\n" \
  "s_waitcnt lgkmcnt(6)\n" \
  "v_mfma_f32_16x16x32_bf16 v[60:63], v[24:27], a[72:75], v[60:63]\n" \
  "ds_read_b64_tr_b16 v[20:21], v4 offset:51216\n" \
  "v_mfma_f32_16x16x32_bf16 v[80:83], v[24:27], a[76:79], v[80:83]\n" \
  "ds_read_b64_tr_b16 v[22:23], v4 offset:52496\n" \
  "s_waitcnt lgkmcnt(6)\n" \
  "v_mfma_f32_16x16x32_bf16 v[64:67], v[28:31], a[72:75], v[64:67]\n" \
  "ds_read_b64_tr_b16 v[24:25], v4 offset:51224\n" \
  "v_mfma_f32_16x16x32_bf16 v[84:87], v[28:31], a[76:79], v[84:87]\n" \
  "ds_read_b64_tr_b16 v[26:27], v4 offset:52504\n" \
  "s_waitcnt lgkmcnt(6) vmcnt(10)\n" \
  "v_mfma_f32_16x16x32_bf16 v[48:51], v[12:15], a[80:83], v[48:51]\n" \
  "ds_read_b64_tr_b16 v[28:29], v4 offset:51232\n" \
  "v_mfma_f32_16x16x32_bf16 v[68:71], v[12:15], a[84:87], v[68:71]\n" \
  "ds_read_b64_tr_b16 v[30:31], v4 offset:52512\n" \
  "s_waitcnt lgkmcnt(6)\n" \
  "v_mfma_f32_16x16x32_bf16 v[52:55], v[16:19], a[80:83], v[52:55]\n" \
  "ds_read_b64_tr_b16 v[12:13], v4 offset:56320\n" \
  "global_load_dwordx4 v[96:99], v6, s[60:61] offset:0 sc1\n" \
  "v_mfma_f32_16x16x32_bf16 v[72:75], v[16:19], a[84:87], v[72:75]\n" \
  "ds_read_b64_tr_b16 v[14:15], v4 offset:57600\n" \
  "s_waitcnt lgkmcnt(6)\n" \
  "v_mfma_f32_16x16x32_bf16 v[56:59], v[20:23], a[80:83], v[56:59]\n" \
  "ds_read_b64_tr_b16 v[16:17], v4 offset:56328\n" \
  "v_mfma_f32_16x16x32_bf16 v[76:79], v[20:23], a[84:87], v[76:79]\n" \
  "ds_read_b64_tr_b16 v[18:19], v4 offset:57608\n" \
  "s_waitcnt lgkmcnt(6)\n" \
  "v_mfma_f32_16x16x32_bf16 v[60:63], v[24:27], a[80:83], v[60:63]\n" \
  "ds_read_b64_tr_b16 v[20:21], v4 offset:56336\n" \
  "global_load_dwordx4 v[100:103], v6, s[60:61] offset:1024 sc1\n" \
  "v_mfma_f32_16x16x32_bf16 v[80:83], v[24:27], a[84:87], v[80:83]\n" \
  "ds_read_b64_tr_b16 v[22:23], v4 offset:57616\n" \
  "s_waitcnt lgkmcnt(6)\n" \
  "v_mfma_f32_16x16x32_bf16 v[64:67], v[28:31], a[80:83], v[64:67]\n" \
  "ds_read_b64_tr_b16 v[24:25], v4 offset:56344\n" \
  "v_mfma_f32_16x16x32_bf16 v[84:87], v[28:31], a[84:87], v[84:87]\n" \
  "ds_read_b64_tr_b16 v[26:27], v4 offset:57624\n" \
  "s_waitcnt lgkmcnt(6) vmcnt(10)\n" \
  "v_mfma_f32_16x16x32_bf16 v[48:51], v[12:15], a[88:91], v[48:51]\n" \
  "ds_read_b64_tr_b16 v[28:29], v4 offset:56352\n" \
  "global_load_dwordx4 v[104:107], v7, s[60:61] offset:0 sc1\n" \
  "v_mfma_f32_16x16x32_bf16 v[68:71], v[12:15], a[92:95], v[68:71]\n" \
  "ds_read_b64_tr_b16 v[30:31], v4 offset:57632\n" \
  "s_waitcnt lgkmcnt(6)\n" \
  "v_mfma_f32_16x16x32_bf16 v[52:55], v[16:19], a[88:91], v[52:55]\n" \
  "ds_read_b64_tr_b16 v[12:13], v5 offset:0\n" \
  "v_mfma_f32_16x16x32_bf16 v[72:75], v[16:19], a[92:95], v[72:75]\n" \
  "ds_read_b64_tr_b16 v[14:15], v5 offset:1280\n" \
  "s_waitcnt lgkmcnt(6)\n" \
  "v_mfma_f32_16x16x32_bf16 v[56:59], v[20:23], a[88:91], v[56:59]\n" \
  "ds_read_b64_tr_b16 v[16:17], v5 offset:8\n" \
  "global_load_dwordx4 v[108:111], v7, s[60:61] offset:1024 sc1\n" \
  "v_mfma_f32_16x16x32_bf16 v[76:79], v[20:23], a[92:95], v[76:79]\n" \
  "ds_read_b64_tr_b16 v[18:19], v5 offset:1288\n" \
  "s_waitcnt lgkmcnt(6)\n" \
  "v_mfma_f32_16x16x32_bf16 v[60:63], v[24:27], a[88:91], v[60:63]\n" \
  "ds_read_b64_tr_b16 v[20:21], v5 offset:16\n" \
  "v_mfma_f32_16x16x32_bf16 v[80:83], v[24:27], a[92:95], v[80:83]\n" \
  "ds_read_b64_tr_b16 v[22:23], v5 offset:1296\n" \
  "s_waitcnt lgkmcnt(6)\n" \
  "v_mfma_f32_16x16x32_bf16 v[64:67], v[28:31], a[88:91], v[64:67]\n" \
  "ds_read_b64_tr_b16 v[24:25], v5 offset:24\n" \
  "v_mfma_f32_16x16x32_bf16 v[84:87], v[28:31], a[92:95], v[84:87]\n" \
  "ds_read_b64_tr_b16 v[26:27], v5 offset:1304\n" \
  "s_waitcnt lgkmcnt(6) vmcnt(10)\n" \
  "v_mfma_f32_16x16x32_bf16 v[48:51], v[12:15], a[96:99], v[48:51]\n" \
  "ds_read_b64_tr_b16 v[28:29], v5 offset:32\n" \
  "v_mfma_f32_16x16x32_bf16 v[68:71], v[12:15], a[100:103], v[68:71]\n" \
  "ds_read_b64_tr_b16 v[30:31], v5 offset:1312\n" \
  "s_waitcnt lgkmcnt(6)\n" \
  "v_mfma_f32_16x16x32_bf16 v[52:55], v[16:19], a[96:99], v[52:55]\n" \
  "ds_read_b64_tr_b16 v[12:13], v5 offset:5120\n" \
  "global_load_dwordx4 v[112:115], v6, s[62:63] offset:0 sc1\n" \
  "v_mfma_f32_16x16x32_bf16 v[72:75], v[16:19], a[100:103], v[72:75]\n" \
  "ds_read_b64_tr_b16 v[14:15], v5 offset:6400\n" \
  "s_waitcnt lgkmcnt(6)\n" \
  "v_mfma_f32_16x16x32_bf16 v[56:59], v[20:23], a[96:99], v[56:59]\n" \
  "ds_read_b64_tr_b16 v[16:17], v5 offset:5128\n" \
  "v_mfma_f32_16x16x32_bf16 v[76:79], v[20:23], a[100:103], v[76:79]\n" \
  "ds_read_b64_tr_b16 v[18:19], v5 offset:6408\n" \
  "s_waitcnt lgkmcnt(6)\n" \
  "v_mfma_f32_16x16x32_bf16 v[60:63], v[24:27], a[96:99], v[60:63]\n" \
  "ds_read_b64_tr_b16 v[20:21], v5 offset:5136\n" \
  "global_load_dwordx4 v[116:119], v6, s[62:63] offset:1024 sc1\n" \
  "v_mfma_f32_16x16x32_bf16 v[80:83], v[24:27], a[100:103], v[80:83]\n" \
  "ds_read_b64_tr_b16 v[22:23], v5 offset:6416\n" \
  "s_waitcnt lgkmcnt(6)\n" \
  "v_mfma_f32_16x16x32_bf16 v[64:67], v[28:31], a[96:99], v[64:67]\n" \
  "ds_read_b64_tr_b16 v[24:25], v5 offset:5144\n" \
  "v_mfma_f32_16x16x32_bf16 v[84:87], v[28:31], a[100:103], v[84:87]\n" \
  "ds_read_b64_tr_b16 v[26:27], v5 offset:6424\n" \
  "s_waitcnt lgkmcnt(6) vmcnt(10)\n" \
  "v_mfma_f32_16x16x32_bf16 v[48:51], v[12:15], a[104:107], v[48:51]\n" \
  "ds_read_b64_tr_b16 v[28:29], v5 offset:5152\n" \
  "global_load_dwordx4 v[120:123], v7, s[62:63] offset:0 sc1\n" \
  "v_mfma_f32_16x16x32_bf16 v[68:71], v[12:15], a[108:111], v[68:71]\n" \
  "ds_read_b64_tr_b16 v[30:31], v5 offset:6432\n" \
  "s_waitcnt lgkmcnt(6)\n" \
  "v_mfma_f32_16x16x32_bf16 v[52:55], v[16:19], a[104:107], v[52:55]\n" \
  "ds_read_b64_tr_b16 v[12:13], v5 offset:10240\n" \
  "v_mfma_f32_16x16x32_bf16 v[72:75], v[16:19], a[108:111], v[72:75]\n" \
  "ds_read_b64_tr_b16 v[14:15], v5 offset:11520\n" \
  "s_waitcnt lgkmcnt(6)\n" \
  "v_mfma_f32_16x16x32_bf16 v[56:59], v[20:23], a[104:107], v[56:59]\n" \
  "ds_read_b64_tr_b16 v[16:17], v5 offset:10248\n" \
  "global_load_dwordx4 v[124:127], v7, s[62:63] offset:1024 sc1\n" \
  "v_mfma_f32_16x16x32_bf16 v[76:79], v[20:23], a[108:111], v[76:79]\n" \
  "ds_read_b64_tr_b16 v[18:19], v5 offset:11528\n" \
  "s_waitcnt lgkmcnt(6)\n" \
  "v_mfma_f32_16x16x32_bf16 v[60:63], v[24:27], a[104:107], v[60:63]\n" \
  "ds_read_b64_tr_b16 v[20:21], v5 offset:10256\n" \
  "v_mfma_f32_16x16x32_bf16 v[80:83], v[24:27], a[108:111], v[80:83]\n" \
  "ds_read_b64_tr_b16 v[22:23], v5 offset:11536\n" \
  "s_waitcnt lgkmcnt(6)\n" \
  "v_mfma_f32_16x16x32_bf16 v[64:67], v[28:31], a[104:107], v[64:67]\n" \
  "ds_read_b64_tr_b16 v[24:25], v5 offset:10264\n" \
  "v_mfma_f32_16x16x32_bf16 v[84:87], v[28:31], a[108:111], v[84:87]\n" \
  "ds_read_b64_tr_b16 v[26:27], v5 offset:11544\n" \
  "s_waitcnt lgkmcnt(6) vmcnt(10)\n" \
  "v_mfma_f32_16x16x32_bf16 v[48:51], v[12:15], a[112:115], v[48:51]\n" \
  "ds_read_b64_tr_b16 v[28:29], v5 offset:10272\n" \
  "v_mfma_f32_16x16x32_bf16 v[68:71], v[12:15], a[116:119], v[68:71]\n" \
  "ds_read_b64_tr_b16 v[30:31], v5 offset:11552\n" \
  "s_waitcnt lgkmcnt(6)\n" \
  "v_mfma_f32_16x16x32_bf16 v[52:55], v[16:19], a[112:115], v[52:55]\n" \
  "ds_read_b64_tr_b16 v[12:13], v5 offset:15360\n" \
  "v_mfma_f32_16x16x32_bf16 v[72:75], v[16:19], a[116:119], v[72:75]\n" \
  "ds_read_b64_tr_b16 v[14:15], v5 offset:16640\n" \
  "s_waitcnt lgkmcnt(6)\n" \
  "v_mfma_f32_16x16x32_bf16 v[56:59], v[20:23], a[112:115], v[56:59]\n" \
  "ds_read_b64_tr_b16 v[16:17], v5 offset:15368\n" \
  "v_mfma_f32_16x16x32_bf16 v[76:79], v[20:23], a[116:119], v[76:79]\n" \
  "ds_read_b64_tr_b16 v[18:19], v5 offset:16648\n" \
  "s_waitcnt lgkmcnt(6)\n" \
  "v_mfma_f32_16x16x32_bf16 v[60:63], v[24:27], a[112:115], v[60:63]\n" \
  "ds_read_b64_tr_b16 v[20:21], v5 offset:15376\n" \
  "v_mfma_f32_16x16x32_bf16 v[80:83], v[24:27], a[116:119], v[80:83]\n" \
  "ds_read_b64_tr_b16 v[22:23], v5 offset:16656\n" \
  "s_waitcnt lgkmcnt(6)\n" \
  "v_mfma_f32_16x16x32_bf16 v[64:67], v[28:31], a[112:115], v[64:67]\n" \
  "ds_read_b64_tr_b16 v[24:25], v5 offset:15384\n" \
  "v_mfma_f32_16x16x32_bf16 v[84:87], v[28:31], a[116:119], v[84:87]\n" \
  "ds_read_b64_tr_b16 v[26:27], v5 offset:16664\n" \
  "s_waitcnt lgkmcnt(6) vmcnt(8)\n" \
  "v_mfma_f32_16x16x32_bf16 v[48:51], v[12:15], a[120:123], v[48:51]\n" \
  "ds_read_b64_tr_b16 v[28:29], v5 offset:15392\n" \
  "v_mfma_f32_16x16x32_bf16 v[68:71], v[12:15], a[124:127], v[68:71]\n" \
  "ds_read_b64_tr_b16 v[30:31], v5 offset:16672\n" \
  "s_waitcnt lgkmcnt(6)\n" \
  "v_mfma_f32_16x16x32_bf16 v[52:55], v[16:19], a[120:123], v[52:55]\n" \
  "ds_read_b64_tr_b16 v[12:13], v5 offset:26112\n" \
  "v_mfma_f32_16x16x32_bf16 v[72:75], v[16:19], a[124:127], v[72:75]\n" \
  "ds_read_b64_tr_b16 v[14:15], v5 offset:27392\n" \
  "s_waitcnt lgkmcnt(6)\n" \
  "v_mfma_f32_16x16x32_bf16 v[56:59], v[20:23], a[120:123], v[56:59]\n" \
  "ds_read_b64_tr_b16 v[16:17], v5 offset:26120\n" \
  "v_mfma_f32_16x16x32_bf16 v[76:79], v[20:23], a[124:127], v[76:79]\n" \
  "ds_read_b64_tr_b16 v[18:19], v5 offset:27400\n" \
  "s_waitcnt lgkmcnt(6)\n" \
  "v_mfma_f32_16x16x32_bf16 v[60:63], v[24:27], a[120:123], v[60:63]\n" \
  "ds_read_b64_tr_b16 v[20:21], v5 offset:26128\n" \
  "v_mfma_f32_16x16x32_bf16 v[80:83], v[24:27], a[124:127], v[80:83]\n" \
  "ds_read_b64_tr_b16 v[22:23], v5 offset:27408\n" \
  "s_waitcnt lgkmcnt(6)\n" \
  "v_mfma_f32_16x16x32_bf16 v[64:67], v[28:31], a[120:123], v[64:67]\n" \
  "ds_read_b64_tr_b16 v[24:25], v5 offset:26136\n" \
  "v_mfma_f32_16x16x32_bf16 v[84:87], v[28:31], a[124:127], v[84:87]\n" \
  "ds_read_b64_tr_b16 v[26:27], v5 offset:27416\n" \
  "s_waitcnt lgkmcnt(6) vmcnt(6)\n" \
  "v_mfma_f32_16x16x32_bf16 v[48:51], v[12:15], v[96:99], v[48:51]\n" \
  "ds_read_b64_tr_b16 v[28:29], v5 offset:26144\n" \
  "v_mfma_f32_16x16x32_bf16 v[68:71], v[12:15], v[100:103], v[68:71]\n" \
  "ds_read_b64_tr_b16 v[30:31], v5 offset:27424\n" \
  "s_waitcnt lgkmcnt(6)\n" \
  "v_mfma_f32_16x16x32_bf16 v[52:55], v[16:19], v[96:99], v[52:55]\n" \
  "ds_read_b64_tr_b16 v[12:13], v5 offset:31232\n" \
  "v_mfma_f32_16x16x32_bf16 v[72:75], v[16:19], v[100:103], v[72:75]\n" \
  "ds_read_b64_tr_b16 v[14:15], v5 offset:32512\n" \
  "s_waitcnt lgkmcnt(6)\n" \
  "v_mfma_f32_16x16x32_bf16 v[56:59], v[20:23], v[96:99], v[56:59]\n" \
  "ds_read_b64_tr_b16 v[16:17], v5 offset:31240\n" \
  "v_mfma_f32_16x16x32_bf16 v[76:79], v[20:23], v[100:103], v[76:79]\n" \
  "ds_read_b64_tr_b16 v[18:19], v5 offset:32520\n" \
  "s_waitcnt lgkmcnt(6)\n" \
  "v_mfma_f32_16x16x32_bf16 v[60:63], v[24:27], v[96:99], v[60:63]\n" \
  "ds_read_b64_tr_b16 v[20:21], v5 offset:31248\n" \
  "v_mfma_f32_16x16x32_bf16 v[80:83], v[24:27], v[100:103], v[80:83]\n" \
  "ds_read_b64_tr_b16 v[22:23], v5 offset:32528\n" \
  "s_waitcnt lgkmcnt(6)\n" \
  "v_mfma_f32_16x16x32_bf16 v[64:67], v[28:31], v[96:99], v[64:67]\n" \
  "ds_read_b64_tr_b16 v[24:25], v5 offset:31256\n" \
  "v_mfma_f32_16x16x32_bf16 v[84:87], v[28:31], v[100:103], v[84:87]\n" \
  "ds_read_b64_tr_b16 v[26:27], v5 offset:32536\n" \
  "s_waitcnt lgkmcnt(6) vmcnt(4)\n" \
  "v_mfma_f32_16x16x32_bf16 v[48:51], v[12:15], v[104:107], v[48:51]\n" \
  "ds_read_b64_tr_b16 v[28:29], v5 offset:31264\n" \
  "v_mfma_f32_16x16x32_bf16 v[68:71], v[12:15], v[108:111], v[68:71]\n" \
  "ds_read_b64_tr_b16 v[30:31], v5 offset:32544\n" \
  "s_waitcnt lgkmcnt(6)\n" \
  "v_mfma_f32_16x16x32_bf16 v[52:55], v[16:19], v[104:107], v[52:55]\n" \
  "ds_read_b64_tr_b16 v[12:13], v5 offset:36352\n" \
  "v_mfma_f32_16x16x32_bf16 v[72:75], v[16:19], v[108:111], v[72:75]\n" \
  "ds_read_b64_tr_b16 v[14:15], v5 offset:37632\n" \
  "s_waitcnt lgkmcnt(6)\n" \
  "v_mfma_f32_16x16x32_bf16 v[56:59], v[20:23], v[104:107], v[56:59]\n" \
  "ds_read_b64_tr_b16 v[16:17], v5 offset:36360\n" \
  "v_mfma_f32_16x16x32_bf16 v[76:79], v[20:23], v[108:111], v[76:79]\n" \
  "ds_read_b64_tr_b16 v[18:19], v5 offset:37640\n" \
  "s_waitcnt lgkmcnt(6)\n" \
  "v_mfma_f32_16x16x32_bf16 v[60:63], v[24:27], v[104:107], v[60:63]\n" \
  "ds_read_b64_tr_b16 v[20:21], v5 offset:36368\n" \
  "v_mfma_f32_16x16x32_bf16 v[80:83], v[24:27], v[108:111], v[80:83]\n" \
  "ds_read_b64_tr_b16 v[22:23], v5 offset:37648\n" \
  "s_waitcnt lgkmcnt(6)\n" \
  "v_mfma_f32_16x16x32_bf16 v[64:67], v[28:31], v[104:107], v[64:67]\n" \
  "ds_read_b64_tr_b16 v[24:25], v5 offset:36376\n" \
  "v_mfma_f32_16x16x32_bf16 v[84:87], v[28:31], v[108:111], v[84:87]\n" \
  "ds_read_b64_tr_b16 v[26:27], v5 offset:37656\n" \
  "s_waitcnt lgkmcnt(6) vmcnt(2)\n" \
  "v_mfma_f32_16x16x32_bf16 v[48:51], v[12:15], v[112:115], v[48:51]\n" \
  "ds_read_b64_tr_b16 v[28:29], v5 offset:36384\n" \
  "v_mfma_f32_16x16x32_bf16 v[68:71], v[12:15], v[116:119], v[68:71]\n" \
  "ds_read_b64_tr_b16 v[30:31], v5 offset:37664\n" \
  "s_waitcnt lgkmcnt(6)\n" \
  "v_mfma_f32_16x16x32_bf16 v[52:55], v[16:19], v[112:115], v[52:55]\n" \
  "ds_read_b64_tr_b16 v[12:13], v5 offset:41472\n" \
  "v_mfma_f32_16x16x32_bf16 v[72:75], v[16:19], v[116:119], v[72:75]\n" \
  "ds_read_b64_tr_b16 v[14:15], v5 offset:42752\n" \
  "s_waitcnt lgkmcnt(6)\n" \
  "v_mfma_f32_16x16x32_bf16 v[56:59], v[20:23], v[112:115], v[56:59]\n" \
  "ds_read_b64_tr_b16 v[16:17], v5 offset:41480\n" \
  "v_mfma_f32_16x16x32_bf16 v[76:79], v[20:23], v[116:119], v[76:79]\n" \
  "ds_read_b64_tr_b16 v[18:19], v5 offset:42760\n" \
  "s_waitcnt lgkmcnt(6)\n" \
  "v_mfma_f32_16x16x32_bf16 v[60:63], v[24:27], v[112:115], v[60:63]\n" \
  "ds_read_b64_tr_b16 v[20:21], v5 offset:41488\n" \
  "v_mfma_f32_16x16x32_bf16 v[80:83], v[24:27], v[116:119], v[80:83]\n" \
  "ds_read_b64_tr_b16 v[22:23], v5 offset:42768\n" \
  "s_waitcnt lgkmcnt(6)\n" \
  "v_mfma_f32_16x16x32_bf16 v[64:67], v[28:31], v[112:115], v[64:67]\n" \
  "ds_read_b64_tr_b16 v[24:25], v5 offset:41496\n" \
  "v_mfma_f32_16x16x32_bf16 v[84:87], v[28:31], v[116:119], v[84:87]\n" \
  "ds_read_b64_tr_b16 v[26:27], v5 offset:42776\n" \
  "s_waitcnt lgkmcnt(6) vmcnt(0)\n" \
  "v_mfma_f32_16x16x32_bf16 v[48:51], v[12:15], v[120:123], v[48:51]\n" \
  "ds_read_b64_tr_b16 v[28:29], v5 offset:41504\n" \
  "v_mfma_f32_16x16x32_bf16 v[68:71], v[12:15], v[124:127], v[68:71]\n" \
  "ds_read_b64_tr_b16 v[30:31], v5 offset:42784\n" \
  "s_waitcnt lgkmcnt(6)\n" \
  "v_mfma_f32_16x16x32_bf16 v[52:55], v[16:19], v[120:123], v[52:55]\n" \
  "v_mfma_f32_16x16x32_bf16 v[72:75], v[16:19], v[124:127], v[72:75]\n" \
  "s_waitcnt lgkmcnt(4)\n" \
  "v_mfma_f32_16x16x32_bf16 v[56:59], v[20:23], v[120:123], v[56:59]\n" \
  "v_mfma_f32_16x16x32_bf16 v[76:79], v[20:23], v[124:127], v[76:79]\n" \
  "s_waitcnt lgkmcnt(2)\n" \
  "v_mfma_f32_16x16x32_bf16 v[60:63], v[24:27], v[120:123], v[60:63]\n" \
  "v_mfma_f32_16x16x32_bf16 v[80:83], v[24:27], v[124:127], v[80:83]\n" \
  "s_waitcnt lgkmcnt(0)\n" \
  "v_mfma_f32_16x16x32_bf16 v[64:67], v[28:31], v[120:123], v[64:67]\n" \
  "v_mfma_f32_16x16x32_bf16 v[84:87], v[28:31], v[124:127], v[84:87]\n" \
  "s_nop 7\n" \
  ""
#define SLOW_PATHS \
  "LK1_s:\n" \
  "ds_read_b32 v11, v8 offset:4\n" \
  "s_sub_i32 s75, s75, 1\n" \
  "s_cmp_lt_i32 s75, 0\n" \
  "s_cbranch_scc1 LK1_k\n" \
  "s_waitcnt lgkmcnt(0)\n" \
  "v_cmp_gt_u32_e32 vcc, 1, v11\n" \
  "s_cbranch_vccnz LK1_s\n" \
  "s_branch LK1_k\n" \
  "LK2_s:\n" \
  "ds_read_b32 v11, v8 offset:8\n" \
  "s_sub_i32 s75, s75, 1\n" \
  "s_cmp_lt_i32 s75, 0\n" \
  "s_cbranch_scc1 LK2_k\n" \
  "s_waitcnt lgkmcnt(0)\n" \
  "v_cmp_gt_u32_e32 vcc, 1, v11\n" \
  "s_cbranch_vccnz LK2_s\n" \
  "s_branch LK2_k\n" \
  "LK3_s:\n" \
  "ds_read_b32 v11, v8 offset:12\n" \
  "s_sub_i32 s75, s75, 1\n" \
  "s_cmp_lt_i32 s75, 0\n" \
  "s_cbranch_scc1 LK3_k\n" \
  "s_waitcnt lgkmcnt(0)\n" \
  "v_cmp_gt_u32_e32 vcc, 1, v11\n" \
  "s_cbranch_vccnz LK3_s\n" \
  "s_branch LK3_k\n" \
  "LK4_s:\n" \
  "ds_read_b32 v11, v8 offset:16\n" \
  "s_sub_i32 s75, s75, 1\n" \
  "s_cmp_lt_i32 s75, 0\n" \
  "s_cbranch_scc1 LK4_k\n" \
  "s_waitcnt lgkmcnt(0)\n" \
  "v_cmp_gt_u32_e32 vcc, 1, v11\n" \
  "s_cbranch_vccnz LK4_s\n" \
  "s_branch LK4_k\n" \
  "LK5_s:\n" \
  "ds_read_b32 v11, v8 offset:20\n" \
  "s_sub_i32 s75, s75, 1\n" \
  "s_cmp_lt_i32 s75, 0\n" \
  "s_cbranch_scc1 LK5_k\n" \
  "s_waitcnt lgkmcnt(0)\n" \
  "v_cmp_gt_u32_e32 vcc, 1, v11\n" \
  "s_cbranch_vccnz LK5_s\n" \
  "s_branch LK5_k\n" \
  "LK6_s:\n" \
  "ds_read_b32 v11, v8 offset:24\n" \
  "s_sub_i32 s75, s75, 1\n" \
  "s_cmp_lt_i32 s75, 0\n" \
  "s_cbranch_scc1 LK6_k\n" \
  "s_waitcnt lgkmcnt(0)\n" \
  "v_cmp_gt_u32_e32 vcc, 1, v11\n" \
  "s_cbranch_vccnz LK6_s\n" \
  "s_branch LK6_k\n" \
  "LK7_s:\n" \
  "ds_read_b32 v11, v8 offset:28\n" \
  "s_sub_i32 s75, s75, 1\n" \
  "s_cmp_lt_i32 s75, 0\n" \
  "s_cbranch_scc1 LK7_k\n" \
  "s_waitcnt lgkmcnt(0)\n" \
  "v_cmp_gt_u32_e32 vcc, 1, v11\n" \
  "s_cbranch_vccnz LK7_s\n" \
  "s_branch LK7_k\n" \
  "LK8_s:\n" \
  "ds_read_b32 v11, v8 offset:32\n" \
  "s_sub_i32 s75, s75, 1\n" \
  "s_cmp_lt_i32 s75, 0\n" \
  "s_cbranch_scc1 LK8_k\n" \
  "s_waitcnt lgkmcnt(0)\n" \
  "v_cmp_gt_u32_e32 vcc, 1, v11\n" \
  "s_cbranch_vccnz LK8_s\n" \
  "s_branch LK8_k\n" \
  "LK9_s:\n" \
  "ds_read_b32 v11, v8 offset:36\n" \
  "s_sub_i32 s75, s75, 1\n" \
  "s_cmp_lt_i32 s75, 0\n" \
  "s_cbranch_scc1 LK9_k\n" \
  "s_waitcnt lgkmcnt(0)\n" \
  "v_cmp_gt_u32_e32 vcc, 1, v11\n" \
  "s_cbranch_vccnz LK9_s\n" \
  "s_branch LK9_k\n" \
  "LQa_s:\n" \
  "ds_read_b32 v11, v8 offset:40\n" \
  "s_sub_i32 s75, s75, 1\n" \
  "s_cmp_lt_i32 s75, 0\n" \
  "s_cbranch_scc1 LQa_k\n" \
  "s_waitcnt lgkmcnt(0)\n" \
  "v_cmp_gt_u32_e32 vcc, 1, v11\n" \
  "s_cbranch_vccnz LQa_s\n" \
  "s_branch LQa_k\n" \
  "LQb_s:\n" \
  "ds_read_b32 v11, v8 offset:40\n" \
  "s_sub_i32 s75, s75, 1\n" \
  "s_cmp_lt_i32 s75, 0\n" \
  "s_cbranch_scc1 LQb_k\n" \
  "s_waitcnt lgkmcnt(0)\n" \
  "v_cmp_gt_u32_e32 vcc, 1, v11\n" \
  "s_cbranch_vccnz LQb_s\n" \
  "s_branch LQb_k\n" \
  "LQ11_s:\n" \
  "ds_read_b32 v11, v8 offset:44\n" \
  "s_sub_i32 s75, s75, 1\n" \
  "s_cmp_lt_i32 s75, 0\n" \
  "s_cbranch_scc1 LQ11_k\n" \
  "s_waitcnt lgkmcnt(0)\n" \
  "v_cmp_gt_u32_e32 vcc, 1, v11\n" \
  "s_cbranch_vccnz LQ11_s\n" \
  "s_branch LQ11_k\n" \
  "LQ12_s:\n" \
  "ds_read_b32 v11, v8 offset:48\n" \
  "s_sub_i32 s75, s75, 1\n" \
  "s_cmp_lt_i32 s75, 0\n" \
  "s_cbranch_scc1 LQ12_k\n" \
  "s_waitcnt lgkmcnt(0)\n" \
  "v_cmp_gt_u32_e32 vcc, 1, v11\n" \
  "s_cbranch_vccnz LQ12_s\n" \
  "s_branch LQ12_k\n" \
  "LQ13_s:\n" \
  "ds_read_b32 v11, v8 offset:52\n" \
  "s_sub_i32 s75, s75, 1\n" \
  "s_cmp_lt_i32 s75, 0\n" \
  "s_cbranch_scc1 LQ13_k\n" \
  "s_waitcnt lgkmcnt(0)\n" \
  "v_cmp_gt_u32_e32 vcc, 1, v11\n" \
  "s_cbranch_vccnz LQ13_s\n" \
  "s_branch LQ13_k\n" \
  "LQ14_s:\n" \
  "ds_read_b32 v11, v8 offset:56\n" \
  "s_sub_i32 s75, s75, 1\n" \
  "s_cmp_lt_i32 s75, 0\n" \
  "s_cbranch_scc1 LQ14_k\n" \
  "s_waitcnt lgkmcnt(0)\n" \
  "v_cmp_gt_u32_e32 vcc, 1, v11\n" \
  "s_cbranch_vccnz LQ14_s\n" \
  "s_branch LQ14_k\n" \
  "LQ15_s:\n" \
  "ds_read_b32 v11, v8 offset:60\n" \
  "s_sub_i32 s75, s75, 1\n" \
  "s_cmp_lt_i32 s75, 0\n" \
  "s_cbranch_scc1 LQ15_k\n" \
  "s_waitcnt lgkmcnt(0)\n" \
  "v_cmp_gt_u32_e32 vcc, 1, v11\n" \
  "s_cbranch_vccnz LQ15_s\n" \
  "s_branch LQ15_k\n" \
  "LO_s:\n" \
  "ds_read_b32 v4, v8 offset:136\n" \
  "s_sub_i32 s75, s75, 1\n" \
  "s_cmp_lt_i32 s75, 0\n" \
  "s_cbranch_scc1 LO_k\n" \
  "s_waitcnt lgkmcnt(0)\n" \
  "v_cmp_gt_u32_e32 vcc, 1, v4\n" \
  "s_cbranch_vccnz LO_s\n" \
  "s_branch LO_k\n" \
  "LTG_s:\n" \
  "s_mov_b32 s75, 0x20000\n" \
  "LTG_l:\n" \
  "global_load_dwordx2 v[38:39], v8, s[64:65] sc1\n" \
  "s_sub_u32 s75, s75, 1\n" \
  "s_waitcnt vmcnt(0)\n" \
  "s_cbranch_scc1 LTG_k\n" \
  "v_cmp_ne_u64_e32 vcc, s[66:67], v[38:39]\n" \
  "s_cbranch_vccz LTG_k\n" \
  "s_sleep 1\n" \
  "s_branch LTG_l\n" \
  ""
#define KVP1 \
  "ds_read_b64_tr_b16 v[40:41], %[vm] offset:0\n" \
  "ds_read_b64_tr_b16 v[44:45], %[ve] offset:0\n" \
  "ds_read_b64_tr_b16 v[42:43], %[vm] offset:336\n" \
  "ds_read_b64_tr_b16 v[46:47], %[ve] offset:336\n" \
  "ds_read_b64_tr_b16 v[48:49], %[vm] offset:16384\n" \
  "ds_read_b64_tr_b16 v[52:53], %[ve] offset:16384\n" \
  "ds_read_b64_tr_b16 v[50:51], %[vm] offset:16720\n" \
  "ds_read_b64_tr_b16 v[54:55], %[ve] offset:16720\n" \
  "ds_read_b64_tr_b16 v[56:57], %[vm] offset:32768\n" \
  "ds_read_b64_tr_b16 v[60:61], %[ve] offset:32768\n" \
  "ds_read_b64_tr_b16 v[58:59], %[vm] offset:33104\n" \
  "ds_read_b64_tr_b16 v[62:63], %[ve] offset:33104\n" \
  "ds_read_b64_tr_b16 v[64:65], %[vm] offset:49152\n" \
  "ds_read_b64_tr_b16 v[68:69], %[ve] offset:49152\n" \
  "ds_read_b64_tr_b16 v[66:67], %[vm] offset:49488\n" \
  "ds_read_b64_tr_b16 v[70:71], %[ve] offset:49488\n" \
  ""
#define KVP2 \
  "ds_read_b64_tr_b16 v[72:73], %[pa] offset:0\n" \
  "ds_read_b64_tr_b16 v[74:75], %[pa] offset:336\n" \
  "ds_read_b64_tr_b16 v[76:77], %[pa] offset:32\n" \
  "ds_read_b64_tr_b16 v[78:79], %[pa] offset:368\n" \
  "ds_read_b64_tr_b16 v[80:81], %[pa] offset:64\n" \
  "ds_read_b64_tr_b16 v[82:83], %[pa] offset:400\n" \
  "ds_read_b64_tr_b16 v[84:85], %[pa] offset:16384\n" \
  "ds_read_b64_tr_b16 v[86:87], %[pa] offset:16720\n" \
  "ds_read_b64_tr_b16 v[88:89], %[pa] offset:16416\n" \
  "ds_read_b64_tr_b16 v[90:91], %[pa] offset:16752\n" \
  "ds_read_b64_tr_b16 v[92:93], %[pa] offset:16448\n" \
  "ds_read_b64_tr_b16 v[94:95], %[pa] offset:16784\n" \
  "ds_read_b64_tr_b16 v[96:97], %[pa] offset:32768\n" \
  "ds_read_b64_tr_b16 v[98:99], %[pa] offset:33104\n" \
  "ds_read_b64_tr_b16 v[100:101], %[pa] offset:32800\n" \
  "ds_read_b64_tr_b16 v[102:103], %[pa] offset:33136\n" \
  "s_waitcnt lgkmcnt(14)\n" \
  "v_mfma_f32_16x16x32_bf16 %[m0], v[72:75], v[40:43], 0\n" \
  "v_mfma_f32_16x16x32_bf16 %[e0], v[72:75], v[44:47], 0\n" \
  "ds_read_b64_tr_b16 v[72:73], %[pa] offset:32832\n" \
  "ds_read_b64_tr_b16 v[74:75], %[pa] offset:33168\n" \
  "s_waitcnt lgkmcnt(14)\n" \
  "v_mfma_f32_16x16x32_bf16 %[m1], v[76:79], v[40:43], 0\n" \
  "v_mfma_f32_16x16x32_bf16 %[e1], v[76:79], v[44:47], 0\n" \
  "ds_read_b64_tr_b16 v[76:77], %[pa] offset:49152\n" \
  "ds_read_b64_tr_b16 v[78:79], %[pa] offset:49488\n" \
  "s_waitcnt lgkmcnt(14)\n" \
  "v_mfma_f32_16x16x32_bf16 %[m2], v[80:83], v[40:43], 0\n" \
  "v_mfma_f32_16x16x32_bf16 %[e2], v[80:83], v[44:47], 0\n" \
  "ds_read_b64_tr_b16 v[80:81], %[pa] offset:49184\n" \
  "ds_read_b64_tr_b16 v[82:83], %[pa] offset:49520\n" \
  "s_waitcnt lgkmcnt(14)\n" \
  "v_mfma_f32_16x16x32_bf16 %[m0], v[84:87], v[48:51], %[m0]\n" \
  "v_mfma_f32_16x16x32_bf16 %[e0], v[84:87], v[52:55], %[e0]\n" \
  "ds_read_b64_tr_b16 v[84:85], %[pa] offset:49216\n" \
  "ds_read_b64_tr_b16 v[86:87], %[pa] offset:49552\n" \
  "s_waitcnt lgkmcnt(14)\n" \
  "v_mfma_f32_16x16x32_bf16 %[m1], v[88:91], v[48:51], %[m1]\n" \
  "v_mfma_f32_16x16x32_bf16 %[e1], v[88:91], v[52:55], %[e1]\n" \
  "s_waitcnt lgkmcnt(12)\n" \
  "v_mfma_f32_16x16x32_bf16 %[m2], v[92:95], v[48:51], %[m2]\n" \
  "v_mfma_f32_16x16x32_bf16 %[e2], v[92:95], v[52:55], %[e2]\n" \
  "s_waitcnt lgkmcnt(10)\n" \
  "v_mfma_f32_16x16x32_bf16 %[m0], v[96:99], v[56:59], %[m0]\n" \
  "v_mfma_f32_16x16x32_bf16 %[e0], v[96:99], v[60:63], %[e0]\n" \
  "s_waitcnt lgkmcnt(8)\n" \
  "v_mfma_f32_16x16x32_bf16 %[m1], v[100:103], v[56:59], %[m1]\n" \
  "v_mfma_f32_16x16x32_bf16 %[e1], v[100:103], v[60:63], %[e1]\n" \
  "s_waitcnt lgkmcnt(6)\n" \
  "v_mfma_f32_16x16x32_bf16 %[m2], v[72:75], v[56:59], %[m2]\n" \
  "v_mfma_f32_16x16x32_bf16 %[e2], v[72:75], v[60:63], %[e2]\n" \
  "s_waitcnt lgkmcnt(4)\n" \
  "v_mfma_f32_16x16x32_bf16 %[m0], v[76:79], v[64:67], %[m0]\n" \
  "v_mfma_f32_16x16x32_bf16 %[e0], v[76:79], v[68:71], %[e0]\n" \
  "s_waitcnt lgkmcnt(2)\n" \
  "v_mfma_f32_16x16x32_bf16 %[m1], v[80:83], v[64:67], %[m1]\n" \
  "v_mfma_f32_16x16x32_bf16 %[e1], v[80:83], v[68:71], %[e1]\n" \
  "s_waitcnt lgkmcnt(0)\n" \
  "v_mfma_f32_16x16x32_bf16 %[m2], v[84:87], v[64:67], %[m2]\n" \
  "v_mfma_f32_16x16x32_bf16 %[e2], v[84:87], v[68:71], %[e2]\n" \
  "s_nop 7\n" \
  ""
#define WO_LOAD \
  "global_load_dwordx4 a[0:3], %[w0], %[s0] offset:-2048\n" \
  "global_load_dwordx4 a[4:7], %[w1], %[s0] offset:-1024\n" \
  "global_load_dwordx4 a[8:11], %[w2], %[s0] offset:0\n" \
  "global_load_dwordx4 a[12:15], %[w3], %[s0] offset:1024\n" \
  "global_load_dwordx4 a[16:19], %[w4], %[s0] offset:2048\n" \
  "global_load_dwordx4 a[20:23], %[w0], %[s1] offset:-2048\n" \
  "global_load_dwordx4 a[24:27], %[w1], %[s1] offset:-1024\n" \
  "global_load_dwordx4 a[28:31], %[w2], %[s1] offset:0\n" \
  "global_load_dwordx4 a[32:35], %[w3], %[s1] offset:1024\n" \
  "global_load_dwordx4 a[36:39], %[w4], %[s1] offset:2048\n" \
  "global_load_dwordx4 a[40:43], %[w0], %[s2] offset:-2048\n" \
  "global_load_dwordx4 a[44:47], %[w1], %[s2] offset:-1024\n" \
  "global_load_dwordx4 a[48:51], %[w2], %[s2] offset:0\n" \
  "global_load_dwordx4 a[52:55], %[w3], %[s2] offset:1024\n" \
  "global_load_dwordx4 a[56:59], %[w4], %[s2] offset:2048\n" \
  "global_load_dwordx4 a[60:63], %[w0], %[s3] offset:-2048\n" \
  "global_load_dwordx4 a[64:67], %[w1], %[s3] offset:-1024\n" \
  "global_load_dwordx4 a[68:71], %[w2], %[s3] offset:0\n" \
  "global_load_dwordx4 a[72:75], %[w3], %[s3] offset:1024\n" \
  "global_load_dwordx4 a[76:79], %[w4], %[s3] offset:2048\n" \
  "global_load_dwordx4 a[80:83], %[w0], %[s4] offset:-2048\n" \
  "global_load_dwordx4 a[84:87], %[w1], %[s4] offset:-1024\n" \
  "global_load_dwordx4 a[88:91], %[w2], %[s4] offset:0\n" \
  "global_load_dwordx4 a[92:95], %[w3], %[s4] offset:1024\n" \
  "global_load_dwordx4 a[96:99], %[w4], %[s4] offset:2048\n" \
  ""
#define WO_STORE \
  "s_waitcnt vmcnt(0)\n" \
  "ds_write_b128 %[a], a[0:3] offset:0\n" \
  "ds_write_b128 %[a], a[4:7] offset:1024\n" \
  "ds_write_b128 %[a], a[8:11] offset:2048\n" \
  "ds_write_b128 %[a], a[12:15] offset:3072\n" \
  "ds_write_b128 %[a], a[16:19] offset:4096\n" \
  "ds_write_b128 %[b], a[20:23] offset:5120\n" \
  "ds_write_b128 %[b], a[24:27] offset:6144\n" \
  "ds_write_b128 %[b], a[28:31] offset:7168\n" \
  "ds_write_b128 %[b], a[32:35] offset:8192\n" \
  "ds_write_b128 %[b], a[36:39] offset:9216\n" \
  "ds_write_b128 %[b], a[40:43] offset:10240\n" \
  "ds_write_b128 %[b], a[44:47] offset:11264\n" \
  "ds_write_b128 %[b], a[48:51] offset:12288\n" \
  "ds_write_b128 %[b], a[52:55] offset:13312\n" \
  "ds_write_b128 %[b], a[56:59] offset:14336\n" \
  "ds_write_b128 %[b], a[60:63] offset:15360\n" \
  "ds_write_b128 %[b], a[64:67] offset:16384\n" \
  "ds_write_b128 %[b], a[68:71] offset:17408\n" \
  "ds_write_b128 %[b], a[72:75] offset:18432\n" \
  "ds_write_b128 %[b], a[76:79] offset:19456\n" \
  "ds_write_b128 %[b], a[80:83] offset:20480\n" \
  "ds_write_b128 %[b], a[84:87] offset:21504\n" \
  "ds_write_b128 %[b], a[88:91] offset:22528\n" \
  "ds_write_b128 %[b], a[92:95] offset:23552\n" \
  "ds_write_b128 %[b], a[96:99] offset:24576\n" \
  ""
#define GATHER \
  "global_load_dwordx4 v[72:75], %[vo], %[k0] offset:0 sc1\n" \
  "global_load_dwordx4 v[76:79], %[vo], %[k0] offset:1792 sc1\n" \
  "global_load_dwordx4 v[80:83], %[vo], %[k1] offset:0 sc1\n" \
  "global_load_dwordx4 v[84:87], %[vo], %[k1] offset:1792 sc1\n" \
  "global_load_dwordx4 v[88:91], %[vo], %[k2] offset:0 sc1\n" \
  "global_load_dwordx4 v[92:95], %[vo], %[k2] offset:1792 sc1\n" \
  "global_load_dwordx4 v[96:99], %[vo], %[k3] offset:0 sc1\n" \
  "global_load_dwordx4 v[100:103], %[vo], %[k3] offset:1792 sc1\n" \
  "s_waitcnt vmcnt(0)\n" \
  "v_mfma_f32_16x16x32_bf16 %[r0], %[al], v[72:75], 0\n" \
  "v_mfma_f32_16x16x32_bf16 %[r1], %[al], v[80:83], 0\n" \
  "v_mfma_f32_16x16x32_bf16 %[r0], %[ah], v[76:79], %[r0]\n" \
  "v_mfma_f32_16x16x32_bf16 %[r1], %[ah], v[84:87], %[r1]\n" \
  "v_mfma_f32_16x16x32_bf16 %[r0], %[al], v[88:91], %[r0]\n" \
  "v_mfma_f32_16x16x32_bf16 %[r1], %[al], v[96:99], %[r1]\n" \
  "v_mfma_f32_16x16x32_bf16 %[r0], %[ah], v[92:95], %[r0]\n" \
  "v_mfma_f32_16x16x32_bf16 %[r1], %[ah], v[100:103], %[r1]\n" \
  "s_nop 7\n" \
  ""
#define HOPS \
  "s_mov_b32 %[b], 0x2000\n" \
  "global_load_dwordx4 v[72:75], %[vo], %[k0] offset:0 sc1\n" \
  "global_load_dwordx4 v[76:79], %[vo], %[k0] offset:1792 sc1\n" \
  "global_load_dwordx4 v[80:83], %[vo], %[k1] offset:0 sc1\n" \
  "global_load_dwordx4 v[84:87], %[vo], %[k1] offset:1792 sc1\n" \
  "global_load_dwordx4 v[88:91], %[vo], %[k2] offset:0 sc1\n" \
  "global_load_dwordx4 v[92:95], %[vo], %[k2] offset:1792 sc1\n" \
  "global_load_dwordx4 v[96:99], %[vo], %[k3] offset:0 sc1\n" \
  "global_load_dwordx4 v[100:103], %[vo], %[k3] offset:1792 sc1\n" \
  "s_sleep 9\n" \
  "global_load_dwordx4 v[40:43], %[vo], %[k0] offset:0 sc1\n" \
  "global_load_dwordx4 v[44:47], %[vo], %[k0] offset:1792 sc1\n" \
  "global_load_dwordx4 v[48:51], %[vo], %[k1] offset:0 sc1\n" \
  "global_load_dwordx4 v[52:55], %[vo], %[k1] offset:1792 sc1\n" \
  "global_load_dwordx4 v[56:59], %[vo], %[k2] offset:0 sc1\n" \
  "global_load_dwordx4 v[60:63], %[vo], %[k2] offset:1792 sc1\n" \
  "global_load_dwordx4 v[64:67], %[vo], %[k3] offset:0 sc1\n" \
  "global_load_dwordx4 v[68:71], %[vo], %[k3] offset:1792 sc1\n" \
  "LG:\n" \
  "s_waitcnt vmcnt(8)\n" \
  "v_mfma_f32_16x16x32_bf16 v[104:107], %[al], v[72:75], 0\n" \
  "v_mfma_f32_16x16x32_bf16 v[108:111], %[al], v[80:83], 0\n" \
  "v_mfma_f32_16x16x32_bf16 v[104:107], %[ah], v[76:79], v[104:107]\n" \
  "v_mfma_f32_16x16x32_bf16 v[108:111], %[ah], v[84:87], v[108:111]\n" \
  "v_mfma_f32_16x16x32_bf16 v[104:107], %[al], v[88:91], v[104:107]\n" \
  "v_mfma_f32_16x16x32_bf16 v[108:111], %[al], v[96:99], v[108:111]\n" \
  "v_mfma_f32_16x16x32_bf16 v[104:107], %[ah], v[92:95], v[104:107]\n" \
  "v_mfma_f32_16x16x32_bf16 v[108:111], %[ah], v[100:103], v[108:111]\n" \
  "s_nop 7\n" \
  "s_nop 2\n" \
  "v_cmp_u_f32_e32 vcc, v104, v108\n" \
  "s_cbranch_vccz LGx\n" \
  "global_load_dwordx4 v[72:75], %[vo], %[k0] offset:0 sc1\n" \
  "global_load_dwordx4 v[76:79], %[vo], %[k0] offset:1792 sc1\n" \
  "global_load_dwordx4 v[80:83], %[vo], %[k1] offset:0 sc1\n" \
  "global_load_dwordx4 v[84:87], %[vo], %[k1] offset:1792 sc1\n" \
  "global_load_dwordx4 v[88:91], %[vo], %[k2] offset:0 sc1\n" \
  "global_load_dwordx4 v[92:95], %[vo], %[k2] offset:1792 sc1\n" \
  "global_load_dwordx4 v[96:99], %[vo], %[k3] offset:0 sc1\n" \
  "global_load_dwordx4 v[100:103], %[vo], %[k3] offset:1792 sc1\n" \
  "s_waitcnt vmcnt(8)\n" \
  "v_mfma_f32_16x16x32_bf16 v[104:107], %[al], v[40:43], 0\n" \
  "v_mfma_f32_16x16x32_bf16 v[108:111], %[al], v[48:51], 0\n" \
  "v_mfma_f32_16x16x32_bf16 v[104:107], %[ah], v[44:47], v[104:107]\n" \
  "v_mfma_f32_16x16x32_bf16 v[108:111], %[ah], v[52:55], v[108:111]\n" \
  "v_mfma_f32_16x16x32_bf16 v[104:107], %[al], v[56:59], v[104:107]\n" \
  "v_mfma_f32_16x16x32_bf16 v[108:111], %[al], v[64:67], v[108:111]\n" \
  "v_mfma_f32_16x16x32_bf16 v[104:107], %[ah], v[60:63], v[104:107]\n" \
  "v_mfma_f32_16x16x32_bf16 v[108:111], %[ah], v[68:71], v[108:111]\n" \
  "s_nop 7\n" \
  "s_nop 2\n" \
  "v_cmp_u_f32_e32 vcc, v104, v108\n" \
  "s_cbranch_vccz LGx\n" \
  "global_load_dwordx4 v[40:43], %[vo], %[k0] offset:0 sc1\n" \
  "global_load_dwordx4 v[44:47], %[vo], %[k0] offset:1792 sc1\n" \
  "global_load_dwordx4 v[48:51], %[vo], %[k1] offset:0 sc1\n" \
  "global_load_dwordx4 v[52:55], %[vo], %[k1] offset:1792 sc1\n" \
  "global_load_dwordx4 v[56:59], %[vo], %[k2] offset:0 sc1\n" \
  "global_load_dwordx4 v[60:63], %[vo], %[k2] offset:1792 sc1\n" \
  "global_load_dwordx4 v[64:67], %[vo], %[k3] offset:0 sc1\n" \
  "global_load_dwordx4 v[68:71], %[vo], %[k3] offset:1792 sc1\n" \
  "s_sub_u32 %[b], %[b], 1\n" \
  "s_cbranch_scc0 LG\n" \
  "LGx:\n" \
  "v_add_f32_e32 v104, v104, v108\n" \
  "v_add_f32_e32 v105, v105, v109\n" \
  "v_add_f32_e32 v106, v106, v110\n" \
  "v_add_f32_e32 v107, v107, v111\n" \
  "v_cvt_pk_bf16_f32 v112, v104, v105\n" \
  "v_cvt_pk_bf16_f32 v113, v106, v107\n" \
  "v_mov_b32_e32 v114, v112\n" \
  "v_mov_b32_e32 v115, v113\n" \
  "s_nop 1\n" \
  "v_permlane16_swap_b32_e32 v112, v114\n" \
  "v_permlane16_swap_b32_e32 v113, v115\n" \
  "s_mov_b32 exec_lo, 0xffff\n" \
  "s_mov_b32 exec_hi, 0xffff\n" \
  "global_store_dwordx4 %[gs], v[112:115], %[kr] offset:-4096 sc1\n" \
  "s_mov_b64 exec, 1\n" \
  "ds_add_u32 %[hs], %[one] offset:20\n" \
  "s_mov_b64 exec, -1\n" \
  "s_sleep 14\n" \
  "s_mov_b32 %[b], 0x2000\n" \
  "global_load_dwordx4 v[72:75], %[lo], %[kr] offset:-4096 sc1\n" \
  "global_load_dwordx4 v[76:79], %[lo], %[kr] offset:-3072 sc1\n" \
  "global_load_dwordx4 v[80:83], %[lo], %[kr] offset:-2048 sc1\n" \
  "global_load_dwordx4 v[84:87], %[lo], %[kr] offset:-1024 sc1\n" \
  "global_load_dwordx4 v[88:91], %[lo], %[kr] offset:0 sc1\n" \
  "global_load_dwordx4 v[92:95], %[lo], %[kr] offset:1024 sc1\n" \
  "global_load_dwordx4 v[96:99], %[lo], %[kr] offset:2048 sc1\n" \
  "global_load_dwordx4 v[100:103], %[lo], %[kr] offset:3072 sc1\n" \
  "s_sleep 9\n" \
  "global_load_dwordx4 v[40:43], %[lo], %[kr] offset:-4096 sc1\n" \
  "global_load_dwordx4 v[44:47], %[lo], %[kr] offset:-3072 sc1\n" \
  "global_load_dwordx4 v[48:51], %[lo], %[kr] offset:-2048 sc1\n" \
  "global_load_dwordx4 v[52:55], %[lo], %[kr] offset:-1024 sc1\n" \
  "global_load_dwordx4 v[56:59], %[lo], %[kr] offset:0 sc1\n" \
  "global_load_dwordx4 v[60:63], %[lo], %[kr] offset:1024 sc1\n" \
  "global_load_dwordx4 v[64:67], %[lo], %[kr] offset:2048 sc1\n" \
  "global_load_dwordx4 v[68:71], %[lo], %[kr] offset:3072 sc1\n" \
  "LC:\n" \
  "s_waitcnt vmcnt(8)\n" \
  "v_max_u32_e32 v104, v72, v73\n" \
  "v_max3_u32 v104, v104, v74, v75\n" \
  "v_max3_u32 v104, v104, v76, v77\n" \
  "v_max3_u32 v104, v104, v78, v79\n" \
  "v_max3_u32 v104, v104, v80, v81\n" \
  "v_max3_u32 v104, v104, v82, v83\n" \
  "v_max3_u32 v104, v104, v84, v85\n" \
  "v_max3_u32 v104, v104, v86, v87\n" \
  "v_max3_u32 v104, v104, v88, v89\n" \
  "v_max3_u32 v104, v104, v90, v91\n" \
  "v_max3_u32 v104, v104, v92, v93\n" \
  "v_max3_u32 v104, v104, v94, v95\n" \
  "v_max3_u32 v104, v104, v96, v97\n" \
  "v_max3_u32 v104, v104, v98, v99\n" \
  "v_max3_u32 v104, v104, v100, v101\n" \
  "v_max3_u32 v104, v104, v102, v103\n" \
  "v_cmp_eq_u32_e32 vcc, -1, v104\n" \
  "s_cbranch_vccz LC72\n" \
  "global_load_dwordx4 v[72:75], %[lo], %[kr] offset:-4096 sc1\n" \
  "global_load_dwordx4 v[76:79], %[lo], %[kr] offset:-3072 sc1\n" \
  "global_load_dwordx4 v[80:83], %[lo], %[kr] offset:-2048 sc1\n" \
  "global_load_dwordx4 v[84:87], %[lo], %[kr] offset:-1024 sc1\n" \
  "global_load_dwordx4 v[88:91], %[lo], %[kr] offset:0 sc1\n" \
  "global_load_dwordx4 v[92:95], %[lo], %[kr] offset:1024 sc1\n" \
  "global_load_dwordx4 v[96:99], %[lo], %[kr] offset:2048 sc1\n" \
  "global_load_dwordx4 v[100:103], %[lo], %[kr] offset:3072 sc1\n" \
  "s_waitcnt vmcnt(8)\n" \
  "v_max_u32_e32 v104, v40, v41\n" \
  "v_max3_u32 v104, v104, v42, v43\n" \
  "v_max3_u32 v104, v104, v44, v45\n" \
  "v_max3_u32 v104, v104, v46, v47\n" \
  "v_max3_u32 v104, v104, v48, v49\n" \
  "v_max3_u32 v104, v104, v50, v51\n" \
  "v_max3_u32 v104, v104, v52, v53\n" \
  "v_max3_u32 v104, v104, v54, v55\n" \
  "v_max3_u32 v104, v104, v56, v57\n" \
  "v_max3_u32 v104, v104, v58, v59\n" \
  "v_max3_u32 v104, v104, v60, v61\n" \
  "v_max3_u32 v104, v104, v62, v63\n" \
  "v_max3_u32 v104, v104, v64, v65\n" \
  "v_max3_u32 v104, v104, v66, v67\n" \
  "v_max3_u32 v104, v104, v68, v69\n" \
  "v_max3_u32 v104, v104, v70, v71\n" \
  "v_cmp_eq_u32_e32 vcc, -1, v104\n" \
  "s_cbranch_vccz LC40\n" \
  "global_load_dwordx4 v[40:43], %[lo], %[kr] offset:-4096 sc1\n" \
  "global_load_dwordx4 v[44:47], %[lo], %[kr] offset:-3072 sc1\n" \
  "global_load_dwordx4 v[48:51], %[lo], %[kr] offset:-2048 sc1\n" \
  "global_load_dwordx4 v[52:55], %[lo], %[kr] offset:-1024 sc1\n" \
  "global_load_dwordx4 v[56:59], %[lo], %[kr] offset:0 sc1\n" \
  "global_load_dwordx4 v[60:63], %[lo], %[kr] offset:1024 sc1\n" \
  "global_load_dwordx4 v[64:67], %[lo], %[kr] offset:2048 sc1\n" \
  "global_load_dwordx4 v[68:71], %[lo], %[kr] offset:3072 sc1\n" \
  "s_sub_u32 %[b], %[b], 1\n" \
  "s_cbranch_scc0 LC\n" \
  "s_waitcnt vmcnt(0)\n" \
  "LC40:\n" \
  "ds_write_b128 %[gl], v[40:43] offset:0\n" \
  "ds_write_b128 %[gl], v[44:47] offset:448\n" \
  "ds_write_b128 %[gl], v[48:51] offset:896\n" \
  "ds_write_b128 %[gl], v[52:55] offset:1344\n" \
  "ds_write_b128 %[gl], v[56:59] offset:3584\n" \
  "ds_write_b128 %[gl], v[60:63] offset:4032\n" \
  "ds_write_b128 %[gl], v[64:67] offset:4480\n" \
  "ds_write_b128 %[gl], v[68:71] offset:4928\n" \
  "s_branch LCx\n" \
  "LC72:\n" \
  "ds_write_b128 %[gl], v[72:75] offset:0\n" \
  "ds_write_b128 %[gl], v[76:79] offset:448\n" \
  "ds_write_b128 %[gl], v[80:83] offset:896\n" \
  "ds_write_b128 %[gl], v[84:87] offset:1344\n" \
  "ds_write_b128 %[gl], v[88:91] offset:3584\n" \
  "ds_write_b128 %[gl], v[92:95] offset:4032\n" \
  "ds_write_b128 %[gl], v[96:99] offset:4480\n" \
  "ds_write_b128 %[gl], v[100:103] offset:4928\n" \
  "LCx:\n" \
  "s_mov_b64 exec, 1\n" \
  "ds_add_u32 %[hs], %[one] offset:8\n" \
  "s_mov_b64 exec, -1\n" \
  "s_waitcnt vmcnt(0)\n" \
  ""
#define LANES() int lane; asm volatile("v_mbcnt_lo_u32_b32 %0, -1, 0\n\tv_mbcnt_hi_u32_b32 %0, -1, %0" : "=v"(lane)); const int i16 = lane & 15, g = lane >> 4, q4 = (lane >> 2) & 3, p4 = lane & 3; (void)i16; (void)g; (void)q4; (void)p4
#define SLP "1"
#define THR
#define VAR 3
#define EM 12
#define LG 5
#define EARLY ((EM >> w) & 1)
#define LATEGATE if (!br) lds_wait(L_CNT + 56, 1); else if (LG >= 4 && (w == 0 || LG == 6)) lds_wait(L_HS + 20, 1); else if (LG != 5) lds_wait(L_HS + 8, 1)
#define FD 4
#define FS 3
#define SCP 1
#define OMOD " nt"
#define ROT2 0
#define PFG 11
#define EP 0
#define RW 2
#define QMIN 7u
#define M_BR(xcd, j) ((xcd) >> 2)
#define M_RT(xcd, j) (((xcd) & 1) * 8 + ((j) & 7))
#define M_HG(xcd, j) ((((xcd) >> 1) & 1) * 4 + ((j) >> 3))
#define XP 3
#define SENT 8
#define AD 0
#define AP 0
#define APM 7
#define TLO 0x3275160887014963ull
#define THI 0x5432819619685430ull
#define OX 5
#define WD 2
#define XSW 8
#define XCD(b) ((((b) & 1) << 2) | (((b) >> 1) & 3))
#define BPF 0
#define BPM ""
#define EQ 0
#define EQH 64
#define F2 0
#define PP 1
#define PPS "3"
#define HP 1
#define TGM 0x0f0f
#define HN 8
#define HS 3
#define HD 0
#define XS 0
#define M_SEL(j) ((j) >> 4)
#define M_JJ(j) ((j) & 15)
#define M_B2(xcd, j) (((xcd) >> 2) ^ (XP & 1))
#define M_P2(xcd, j) (((xcd) & 3) * 4 + ((j) & 3))
#define M_C2(xcd, j) ((j) >> 2)
#define ADLY 0
#define TOFF 0
#define SWAPW 0
#define DMA5 "global_load_lds_dwordx4 %[w0], s[90:91] offset:-2048\n\tglobal_load_lds_dwordx4 %[w1], s[90:91] offset:-1024\n\tglobal_load_lds_dwordx4 %[w2], s[90:91]\n\t" \
             "global_load_lds_dwordx4 %[w3], s[90:91] offset:1024\n\tglobal_load_lds_dwordx4 %[w4], s[90:91] offset:2048\n\t"
#define HTS
#define HTS0
#define HCLOB
#define HTS_OUT()
#define POST "s_mov_b64 exec, 1\n\tds_add_u32 %[c], %[inc]\n\ts_mov_b64 exec, -1\n\tv_add_u32_e32 %[c], 4, %[c]\n\t"
#define WAITP(L) "s_mov_b32 %[b], 0x40000\n" L ":\n\tds_read_b128 v[4:7], %[f]\n\ts_sub_u32 %[b], %[b], 1\n\ts_cbranch_scc1 " L "x\n\ts_waitcnt lgkmcnt(0)\n\t" \
             "v_min3_u32 v4, v4, v5, v6\n\tv_min_u32_e32 v4, v4, v7\n\tv_cmp_gt_u32_e32 vcc, %[n], v4\n\ts_cbranch_vccz " L "x\n\ts_sleep " SLP "\n\ts_branch " L "\n" L "x:\n\t"
struct V5 { u32 v[5]; };
DI void wait4(u32 a, u32 n) { u32 bnd; asm volatile(WAITP("LPw_%=") : [b] "=&s"(bnd) : [f] "v"(a), [n] "s"(n) : "memory", "scc", "vcc", "v4", "v5", "v6", "v7"); }
DI void wgroup(const V5& vw, u32 lds, u64 src, u32 half) {
  asm volatile("s_add_u32 m0, %[l], 2048\n\ts_mov_b64 s[90:91], %[a]\n\t" DMA5 "s_add_u32 m0, %[l], 7168\n\ts_add_u32 s90, s90, %[h]\n\ts_addc_u32 s91, s91, 0\n\t" DMA5
               :: [w0] "v"(vw.v[0]), [w1] "v"(vw.v[1]), [w2] "v"(vw.v[2]), [w3] "v"(vw.v[3]), [w4] "v"(vw.v[4]), [l] "s"(lds), [a] "s"(src), [h] "s"(half) : "memory", "scc", "s90", "s91");
}
#define X4 "global_load_lds_dwordx4 %[x0], s[90:91]\n\tglobal_load_lds_dwordx4 %[x1], s[90:91] offset:1024\n\tglobal_load_lds_dwordx4 %[x2], s[90:91] offset:2048\n\tglobal_load_lds_dwordx4 %[x3], s[90:91] offset:3072\n\t"
#define XN "s_add_u32 m0, m0, 16384\n\ts_add_u32 s90, s90, %[st]\n\ts_addc_u32 s91, s91, 0\n\t"
DI void xgroup(const u32x4& vx, u32 lds, u64 src, u32 step) {
  asm volatile("s_mov_b32 m0, %[l]\n\ts_mov_b64 s[90:91], %[a]\n\t" X4 XN X4 XN X4 XN X4
               :: [x0] "v"(vx[0]), [x1] "v"(vx[1]), [x2] "v"(vx[2]), [x3] "v"(vx[3]), [l] "s"(lds), [a] "s"(src), [st] "s"(step) : "memory", "scc", "s90", "s91");
}
DI void lds_arrive(u32 a, u32 n = 1) { asm volatile("s_waitcnt lgkmcnt(0)\n\ts_mov_b64 exec, 1\n\tds_add_u32 %0, %1\n\ts_mov_b64 exec, -1" :: "v"(a), "v"(n) : "memory"); }
DI void lds_post(u32 a) { asm volatile("s_mov_b64 exec, 1\n\tds_add_u32 %0, %1\n\ts_mov_b64 exec, -1" :: "v"(a), "v"(1u) : "memory"); }
DI void lds_wait(u32 a, u32 n) {
  u32 bnd, t;
  asm volatile("s_mov_b32 %0, 0x40000\nLq_%=:\n\tds_read_b32 %1, %2\n\ts_sub_u32 %0, %0, 1\n\ts_cbranch_scc1 Lr_%=\n\ts_waitcnt lgkmcnt(0)\n\tv_cmp_gt_u32_e32 vcc, %3, %1\n\ts_cbranch_vccz Lr_%=\n\ts_sleep " SLP "\n\ts_branch Lq_%=\nLr_%=:"
               : "=&s"(bnd), "=&v"(t) : "v"(a), "s"(n) : "memory", "scc", "vcc");
}
DI V5 wlanes(int lane, int stride) {
  V5 r;
#pragma unroll
  for (int i = 0; i < 5; ++i) { int G = 64 * i + lane, q = (G * 6554) >> 16, c = G - 10 * q; int kr = (q & 19) | ((q & 4) << 1) | ((q & 8) >> 1); r.v[i] = kr * stride + c * 16 - (i - 2) * 1024; }
  return r;
}
DI void wait_flags(const char* p, int n, u64 tag, int lane, int stride = 128) {
#pragma nounroll
  for (u32 s = 0; s < SPIN_CAP; ++s) {
    u64 v = tag;
    if (lane < n) v = __hip_atomic_load((GAS u64*)(p + lane * stride), RLX_AGENT);
    if (__all(v == tag)) break;
    __builtin_amdgcn_s_sleep(FS);
  }
  asm volatile("" ::: "memory");
}
DI void st16(char* base, u32 off, u32x4 v) { asm volatile("global_store_dwordx4 %0, %1, %2 sc1\n\ts_nop 1" :: "v"(off), "v"(v), "s"(base) : "memory"); }
DI void scrub(char* base, u32 off, int n) {
  const u32x4 v = {~0u, ~0u, ~0u, ~0u};
#pragma nounroll
  for (int i = 0; i < n; ++i) st16(base + i * 1024, off, v);
}
DI void put_flag(char* p, u64 tag) {
  u32 o;
  asm volatile("v_mbcnt_lo_u32_b32 %0, -1, 0\n\tv_lshlrev_b32_e32 %0, 3, %0\n\ts_mov_b64 exec, 0xffff\n\tglobal_store_dwordx2 %0, %1, %2 sc1\n\ts_mov_b64 exec, -1" : "=&v"(o) : "v"(tag), "s"(p) : "memory");
}
DI void drain() { asm volatile("s_waitcnt vmcnt(0)" ::: "memory"); }

#define UNPACK() float v[40]; _Pragma("unroll") for (int i = 0; i < 16; ++i) { v[i] = c0[i]; v[16 + i] = c1[i]; } _Pragma("unroll") for (int i = 0; i < 8; ++i) v[32 + i] = c2[i]

#define IDS() int b_ = b, w_ = wv; asm volatile("" : "+s"(b_), "+s"(w_)); const int xcd = XCD(b_), j = b_ >> 3, br = M_BR(xcd, j), w = w_ & 3, rt = M_RT(xcd, j), hg = M_HG(xcd, j), rstr = 2 - br; \
  char* const hp = ws + W_HALO + (rt * 8 + hg) * 3072; \
  char* const fo = ws + W_FO + ((br * 16 + rt) * 64 + hg) * 128; \
  char* const obase = ws + W_O + (size_t)br * 16 * 163840; \
  char* const op = obase + (size_t)rt * 163840; \
  char* const kp = ws + W_KVP + (size_t)(hg * 16 + rt) * 7168; \
  const int b2 = M_B2(xcd, j), p2 = M_P2(xcd, j), c2g = M_C2(xcd, j); char* const ip = ws + W_O + (size_t)(b2 * 16 + p2) * 163840; (void)ip; \
  const u32 rot2 = ROT2; \
  const u64 wosrc = wo + c2g * 160; \
  const u32 blk = L_X + w * 16384; \
  (void)hp; (void)fo; (void)op; (void)kp; (void)p2; (void)rot2; (void)wosrc; (void)blk; (void)rstr
__global__ __launch_bounds__(512, 2) void fused_kernel(const void*, const void*, const void*, const void*, const void*, const void*, const void*, const void*, void*, void*) {
  __shared__ __attribute__((aligned(16))) char lds[163840];
  const int b = blockIdx.x, xcd = XCD(b), j = b >> 3, br = M_BR(xcd, j);
  u64x4 ka; u64x2 kb;
  asm volatile("s_load_dwordx8 %0, %2, %3\n\ts_load_dwordx4 %1, %2, 0x40" : "=&s"(ka), "=&s"(kb) : "s"(__builtin_amdgcn_kernarg_segment_ptr()), "s"(br * 32), "v"(lds) : "memory");
  u64 wo;
  asm volatile("s_load_dwordx2 %0, %1, %2" : "=&s"(wo) : "s"(__builtin_amdgcn_kernarg_segment_ptr()), "s"(M_B2(xcd, j) * 32 + 16) : "memory");
  const int tid = threadIdx.x ^ SWAPW, wv = rfl(tid >> 6), w = wv & 3;
  u32 pf = min((j & 7) * 8192 + tid * 128, CODE_BYTES);
  if (wv == 0 && (j & 7) < 5) asm volatile("s_getpc_b64 s[76:77]\n\tglobal_load_dword a124, %0, s[76:77]" :: "v"(pf) : "memory", "s76", "s77", "a124");
  const bool isG = wv < 4;
  const int rt = M_RT(xcd, j), hg = M_HG(xcd, j);
  const int rstr = 2 - br;
#define OW 0
  u32 rot_ = (u32)((j & 16 ? THI : TLO) >> (4 * (j & 15))) & 15u;
  if (OW + OX) { rot_ += OW * (xcd & 1) + OX * ((xcd >> 1) & 1); rot_ = rot_ < 10 ? rot_ : rot_ - 10; }
  const u32 rot = rot_;
  V5 vw; u32x4 vXs;
  const u32 rs = rstr * 1280, row0 = br ? 128 * rt : 256 * (rt >> 1) + (rt & 1);
  vXs[0] = ((tid & 63) >> 3) * rs + (((tid ^ ((tid & 63) >> 4)) & 7) << 4); vXs[1] = (vXs[0] ^ 64) + 8 * rs - 1024; vXs[2] = vXs[0] + 16 * rs - 2048; vXs[3] = vXs[1] + 16 * rs - 2048;
  u32 xo0 = row0 * 1280 + rot * 128, xst = rstr * 40960;
  asm volatile("" : "+v"(vXs));
  asm volatile("s_waitcnt lgkmcnt(0)" : "+s"(ka), "+s"(kb), "+s"(wo), "+s"(xo0), "+s"(xst) :: "memory");
  if (ADLY && wv == 4 && !br) __builtin_amdgcn_s_sleep(ADLY);
  if (wv == 4) xgroup(vXs, L_X, ka[0] + xo0, xst);
  if (!isG) {
    int lane = tid & 63;
    asm volatile("" : "+v"(lane));
    vw = wlanes(lane, 3840);
    if (wv == 4) {
      const u64 wo0 = ka[1] + hg * 160 + 1280 + rot * 245760;
      wgroup(vw, 0, wo0, 122880);
      wgroup(vw, 10240, wo0 + 1280, 122880);
    }
  } else {
    ldsw<u32x2>(L_CNT + 8 * tid, (u32x2){0u, 0u});
  }
  asm volatile("s_waitcnt lgkmcnt(0)\n\ts_barrier" ::: "memory");
  char* const out = (char*)kb[0]; char* const ws = (char*)kb[1];
  u64 tag;
  f32x16 c0, c1; f32x8 c2;
  STAMP(15);
  if (!isG) {
    const u64 sXb = ka[0] + (u64)row0 * 1280, sW = ka[1] + hg * 160;
    HTS0;
    V5 vo; u32 pa, vm, ve, a0, a1, a2, go, gs, gl; u32x4 Alo, Ahi;
    const int u = w < 2, hd = 2 * (w & 1) + u;
    __builtin_amdgcn_s_setprio(3);
#pragma nounroll
    for (u32 G = w; G < 14; G += 4) {
      if (G == (w < 2 ? w + 4 : w)) {
        asm volatile("s_load_dwordx2 %0, %1, 0x0\n\ts_waitcnt lgkmcnt(0)" : "=s"(tag) : "s"(ws + W_EPOCH) : "memory");
        LANES();
        vo = wlanes(lane, 1280);
        const u32 kr = (16 * (g & 1) + 4 * q4 + (g >> 1)) * RS, vr = L_X + O_V + kr;
        pa = L_X + (w & 1) * 5376 + u * 80 + kr + 8 * p4; vm = vr + 40 * hd + 8 * p4; ve = vr + (p4 == 0 ? 40 * hd + 32 : 160);
        const int m = 2 * (g & 1) + (g >> 1);
        a0 = (m * 16 + i16) * 16; a1 = 1280 + (m * 6 + i16) * 16; a2 = (g ? 1664 : 1024) + i16 * 16;
        const u32 one = 0x3f80u << ((i16 & 1) * 16); const int jj = (i16 & 7) >> 1;
        const u32x4 sel = {jj == 0 ? one : 0u, jj == 1 ? one : 0u, jj == 2 ? one : 0u, jj == 3 ? one : 0u}, z4 = {0u, 0u, 0u, 0u};
        Alo = i16 < 8 ? sel : z4; Ahi = i16 < 8 ? z4 : sel;
        go = g * 7168 + min(i16, 13) * 16; gs = rt * 512 + (g >> 1) * 256 + i16 * 16; gl = L_KV + (g & 1) * 1792 + (g >> 1) * 224 + min(i16, 13) * 16;
        asm volatile("" : "+v"(vo.v[0]), "+v"(vo.v[1]), "+v"(vo.v[2]), "+v"(vo.v[3]), "+v"(vo.v[4]), "+v"(pa), "+v"(vm), "+v"(ve), "+v"(a0), "+v"(a1), "+v"(a2), "+v"(Alo), "+v"(Ahi), "+v"(go), "+v"(gs), "+v"(gl));
      }
      const bool kv = G < 10;
      u32 p = rot + (kv ? G : 2 * G - 20); p = p < 10 ? p : p - 10;
      const u32 p1 = kv ? p : p == 9 ? 0 : p + 1;
      u64 sa = sW + p * 245760 + (kv ? 1280 : 0), sb = sW + p1 * 245760 + (kv ? 2560 : 0), sx = sXb + p * 128;
      asm volatile("" : "+s"(sa), "+s"(sb), "+s"(sx));
      if (G >= 4) wait4(L_FREE, kv || !br ? G - 3 : max(G - 3, QMIN)); else if (G > 1) lds_wait(L_HS + 16, G - 1);
      if (G) {
      if (kv) {
        xgroup(vXs, L_X + w * 4096, sx, rstr * 40960);
      }
      wgroup(vw, w * 20480, sa, 122880);
      wgroup(vw, w * 20480 + 10240, sb, 122880);
      lds_post(L_HS + 16);
      }
      if (G == 3 && br) asm volatile("s_mov_b32 m0, %2\n\ts_nop 0\n\tglobal_load_lds_dwordx4 %0, %1\n\tglobal_load_lds_dwordx4 %0, %1 offset:1024\n\tglobal_load_lds_dwordx4 %0, %1 offset:2048\n\t"
                                     "s_mov_b64 exec, 0xff\n\tglobal_load_lds_dwordx4 %0, %1 offset:3072\n\ts_mov_b64 exec, -1" :: "v"((tid & 63) * 16), "s"(ka[3] + hg * 3200), "s"(L_WPHI) : "memory");
      HTS;
      if (G == PFG) {
        {
          IDS();
          asm volatile("global_load_dword a120, %0, %1\n\tglobal_load_dword a121, %0, %2\n\tglobal_load_dword a122, %0, %3\n\tglobal_load_dword a123, %0, %4" :: "v"(0u), "s"(op + hg * 20480), "s"(ip),
                       "s"(out + (size_t)(b2 ? 128 * p2 : 256 * (p2 >> 1)) * 2560), "s"(ws + W_F2 - 128) : "memory", "a120", "a121", "a122", "a123");
        }
        asm volatile("global_load_dword a124, %0, %1\n\tglobal_load_dword a125, %0, %1 offset:128\n\tglobal_load_dword a126, %0, %1 offset:256\n\tglobal_load_dword a127, %0, %1 offset:384\n\ts_waitcnt vmcnt(8)"
                     :: "v"(min(tid & 63, 49) * 512), "s"(wo + j * 25600) : "memory", "a124", "a125", "a126", "a127");
      } else drain();
      lds_post(L_CNT + 4 * G);
      if (G == 0) STAMP(14);
      HTS;
      if (G == 3) {
        IDS();
        if (br) { scrub(kp, (tid & 63) * 16, 7); scrub(ws + W_KVR + hg * 8192 + rt * 512, (tid & 31) * 16, 1); } else scrub(hp, (tid & 63) * 16, 3);
      }
      if (G == 7) { IDS(); put_flag(ws + W_FK + (128 - 128 * br + hg * 16 + rt) * 128, tag + 1); }
    }
    HTS_OUT(); STAMP(1);
    IDS();
    LANES();
    auto WO_Q = [&]() {
      u64 s[5];
#pragma unroll
      for (int k = 0; k < 5; ++k) { const u32 c = 2 * rot2 + 5 * w + k; s[k] = wosrc + rfl(c < 20 ? c : c - 20) * 40960; }
      STAMP(4);
      asm volatile(WO_LOAD :: [w0] "v"(vo.v[0]), [w1] "v"(vo.v[1]), [w2] "v"(vo.v[2]), [w3] "v"(vo.v[3]), [w4] "v"(vo.v[4]),
                   [s0] "s"(s[0]), [s1] "s"(s[1]), [s2] "s"(s[2]), [s3] "s"(s[3]), [s4] "s"(s[4]) : "memory", "vcc", "a0","a1","a2","a3","a4","a5","a6","a7","a8","a9","a10","a11","a12","a13","a14","a15","a16","a17","a18","a19","a20","a21","a22","a23","a24","a25","a26","a27","a28","a29","a30","a31","a32","a33","a34","a35","a36","a37","a38","a39","a40","a41","a42","a43","a44","a45","a46","a47","a48","a49","a50","a51","a52","a53","a54","a55","a56","a57","a58","a59","a60","a61","a62","a63","a64","a65","a66","a67","a68","a69","a70","a71","a72","a73","a74","a75","a76","a77","a78","a79","a80","a81","a82","a83","a84","a85","a86","a87","a88","a89","a90","a91","a92","a93","a94","a95","a96","a97","a98","a99","a100","a101","a102","a103","a104","a105","a106","a107","a108","a109","a110","a111","a112","a113","a114","a115","a116","a117","a118","a119","a120","a121","a122","a123","a124","a125","a126","a127");
    };
    if (br) {
      wait4(L_STG, 1);
      STAMP(2);
      f32x32 vf;
      asm volatile(KVP1 "s_waitcnt lgkmcnt(0)" : "=&{v[40:71]}"(vf) : [vm] "v"(vm), [ve] "v"(ve) : "memory");
      if (w == RW) wait_flags(ws + W_FK + hg * 16 * 128, 16, tag + 1, lane);
      if (EARLY && !EP) WO_Q();
      wait4(L_STG, 2 + u);
      STAMP(8);
      f32x4 m0, m1, m2, e0, e1, e2;
      asm volatile(KVP2 : [m0] "=&v"(m0), [m1] "=&v"(m1), [m2] "=&v"(m2), [e0] "=&v"(e0), [e1] "=&v"(e1), [e2] "=&v"(e2) : [pa] "v"(pa), "{v[40:71]}"(vf) : "memory", "v72","v73","v74","v75","v76","v77","v78","v79","v80","v81","v82","v83","v84","v85","v86","v87","v88","v89","v90","v91","v92","v93","v94","v95","v96","v97","v98","v99","v100","v101","v102","v103");
      auto pair16 = [&](const f32x4& a, const f32x4& c) {
        auto r0 = __builtin_amdgcn_permlane16_swap(cvtpk(a[0], a[1]), cvtpk(c[0], c[1]), false, false);
        auto r1 = __builtin_amdgcn_permlane16_swap(cvtpk(a[2], a[3]), cvtpk(c[2], c[3]), false, false);
        return (u32x4){r0[0], r1[0], r0[1], r1[1]};
      };
      char* p = kp + hd * 1792;
      st16(p, a0, pair16(m0, m1));
      const u32x4 s1 = pair16(e0, e1), s2 = pair16(m2, e2);
      if (i16 < 6) st16(p, a1, s1);
      if (lane < 24) st16(p, a2, s2);
      lds_post(L_HS + 12);
      STAMP(9);
      if (EARLY && EP) WO_Q();
    }
    if (w == 3) {
      const u64 wsrc = ka[1] + hg * 160;
      wait4(L_FREE, 11);
      wgroup(vw, 40960, wsrc + (rot + 8) % 10 * 245760, 122880);
      wait4(L_FREE, 12);
      wgroup(vw, 51200, wsrc + (rot + 9) % 10 * 245760, 122880);
      asm volatile("s_waitcnt vmcnt(10)" ::: "memory");
      lds_post(L_CNT + 56);
      drain();
      lds_post(L_CNT + 60);
      STAMP(3);
    } else if (br) {
      if (w == RW) {
        if (RW) lds_wait(L_HS + 12, 4);
        __builtin_amdgcn_s_sleep(PD1);
        STAMP(5);
        const char* kq = ws + W_KVP + (size_t)hg * 16 * 7168 + (rt >> 3) * 3584 + (rt & 7) * 224;
        char* kr = ws + W_KVR + hg * 8192;
        u32 bnd;
        asm volatile(HOPS : [b] "=&s"(bnd)
                     : [vo] "v"(go), [k0] "s"(kq), [k1] "s"(kq + 4 * 7168), [k2] "s"(kq + 8 * 7168), [k3] "s"(kq + 12 * 7168), [al] "v"(Alo), [ah] "v"(Ahi),
                       [gs] "v"(gs), [gl] "v"(gl), [lo] "v"(lane * 16), [kr] "s"(kr + 4096), [hs] "v"(L_HS), [one] "v"(1u)
                     : "memory", "scc", "vcc", "v40","v41","v42","v43","v44","v45","v46","v47","v48","v49","v50","v51","v52","v53","v54","v55","v56","v57","v58","v59","v60","v61","v62","v63","v64","v65","v66","v67","v68","v69","v70","v71","v72","v73","v74","v75","v76","v77","v78","v79","v80","v81","v82","v83","v84","v85","v86","v87","v88","v89","v90","v91","v92","v93","v94","v95","v96","v97","v98","v99","v100","v101","v102","v103","v104","v105","v106","v107","v108","v109","v110","v111","v112","v113","v114","v115");
        STAMP(7);
      }
    } else if (w == 2) {
      const u32 img = L_X + 3 * 16384 + 24 * RS + lane * 16;
      if (rt >= 2) wait_flags(ws + W_FK + (128 + hg * 16 + rt - 2) * 128, 1, tag + 1, lane);
      lds_wait(L_STG + 12, 2);
      const u32x4 r0 = ldsr<u32x4>(img + O_V), r1 = ldsr<u32x4>(img + (lane < 20 ? O_V + 1024 : O_K - 320)), r2 = ldsr<u32x4>(img + O_K + 320);
      st16(hp, lane * 16, r0); st16(hp + 1024, lane * 16, r1); st16(hp + 1664, lane * 16, r2);
      STAMP(5);
      u32x4 h0 = {0u, 0u, 0u, 0u}, h1 = h0, h2 = h0;
      if (rt >= 2) {
#pragma nounroll
        for (int s = 0; s < RETRIES; ++s) {
          __builtin_amdgcn_s_sleep(8);
          asm volatile("global_load_dwordx4 %0, %3, %4 sc1\n\tglobal_load_dwordx4 %1, %3, %4 offset:1024 sc1\n\tglobal_load_dwordx4 %2, %3, %4 offset:1664 sc1\n\ts_waitcnt vmcnt(0)"
                       : "=&v"(h0), "=&v"(h1), "=&v"(h2) : "v"(lane * 16), "s"(hp - 16 * 3072) : "memory");
          const u32x4 mx = __builtin_elementwise_max(__builtin_elementwise_max(h0, h1), h2);
          if (!__any(max(max(mx[0], mx[1]), max(mx[2], mx[3])) == ~0u)) break;
        }
      }
      ldsw<u32x4>(L_SP + lane * 16, h0); ldsw<u32x4>(L_SP + 1024 + lane * 16, h1); ldsw<u32x4>(L_SP + 1664 + lane * 16, h2);
      lds_post(L_HS);
      STAMP(7);
    }
    if (!(br && EARLY)) { LATEGATE; WO_Q(); }
  } else {
    IDS();
    LANES();
    asm volatile("s_load_dwordx2 %0, %1, 0x0\n\ts_waitcnt lgkmcnt(0)" : "=s"(tag) : "s"(ws + W_EPOCH) : "memory");
    STAMP(1);
    const int hf = lane >> 5, t32 = lane & 31;
    if (br) {
      const int reg = (t32 & 3) + 4 * (t32 >> 3);
      const bool ow = ((t32 >> 2) & 1) == hf;
      const u32 ka = L_KV + hf * 3584 + reg * 16;
      u32 so[5];
#pragma unroll
      for (int m = 0; m < 5; ++m) { const int c = 10 * hg + 5 * hf + m; so[m] = (c >> 2) * 8192 + (c & 3) * 256 + (t32 >> 4) * 1024 + i16 * 16; }
      ldsw<u32x4>(L_Z2 + lane * 16, (u32x4){ow ? ka : L_ZERO, (ow && reg < 5) ? ka + 1280 : L_ZERO, ow ? ka + 1792 : L_ZERO, (ow && reg < 5) ? ka + 3072 : L_ZERO});
      ldsw<u32x4>(L_Z2 + 1024 + lane * 16, (u32x4){so[0], so[1], so[2], so[3]});
      ldsw<u32>(L_Z2 + 2048 + lane * 16, so[4]);
    }
    u32 vWa = (16 * (g >> 1) + 4 * (g & 1) + q4) * 160 + 40 * p4, vWc = vWa + 61440;
    u32 vX0 = blk + (i16 >> 3) * 1024 + (i16 & 7) * 128 + ((g ^ ((i16 >> 1) & 7)) << 4), vX1 = vX0 ^ 64, vSt = blk + i16 * RS + 40 * g;
    const u32 a = L_WPHI + q4 * 40;
    asm volatile(KV_BODY : "+{v4}"(vWa), "+{v5}"(vWc), "+{v6}"(vX0), "+{v7}"(vX1), "+{v10}"(vSt)
                 : "{v0}"((p4 & 1) == hf ? a + hf * 1600 + 8 * (2 * (g & 1) + (p4 >> 1)) : L_ZERO), "{v9}"(p4 == g ? a + g * 800 + 32 : L_ZERO), "{v1}"(blk + lane * RS),
                   "{v8}"(L_CNT), "{v3}"(lane == 0 ? L_FREE + 4 * w : L_DUM + 4 * lane), "{v2}"(1u), MIDIN "{s70}"(0x3fb8aa3bu), "{s74}"(br), "{s[68:69]}"(op + w * 2048)
                 : "memory", "scc", "vcc", "s75", MCLOB "v11","v12","v13","v14","v15","v16","v17","v18","v19","v20","v21","v22","v23","v24","v25","v26","v27","v28","v29","v30","v31","v32","v33","v34","v35","v36","v37","v38","v39","v40","v41","v42","v43","v44","v45","v46","v47","v48","v49","v50","v51","v52","v53","v54","v55","v56","v57","v58","v59","v60","v61","v62","v63","v64","v65","v66","v67","v68","v69","v70","v71","v72","v73","v74","v75","v76","v77","v78","v79","v80","v81","v82","v83","v84","v85","v86","v87","v88","v89","v90","v91","v92","v93","v94","v95","v96","v97","v98","v99","v100","v101","v102","v103","v104","v105","v106","v107","v108","v109","v110","v111","v112","v113","v114","v115","v116","v117","v118","v119","v120","v121","v122","v123","v124","v125","v126","v127", "a0","a1","a2","a3","a4","a5","a6","a7","a8","a9","a10","a11","a12","a13","a14","a15","a16","a17","a18","a19","a20","a21","a22","a23","a24","a25","a26","a27","a28","a29","a30","a31","a32","a33","a34","a35","a36","a37","a38","a39","a40","a41","a42","a43","a44","a45","a46","a47","a48","a49","a50","a51","a52","a53","a54","a55","a56","a57","a58","a59","a60","a61","a62","a63","a64","a65","a66","a67","a68","a69","a70","a71","a72","a73","a74","a75","a76","a77","a78","a79","a80","a81","a82","a83","a84","a85","a86","a87","a88","a89","a90","a91","a92","a93","a94","a95","a96","a97","a98","a99","a100","a101","a102","a103","a104","a105","a106","a107","a108","a109","a110","a111","a112","a113","a114","a115","a116","a117","a118","a119","a120","a121","a122","a123","a124","a125","a126","a127");
    TS_OUT();
    if (br) STAMP(9);
    MID_OUT();
    if (br) {
      IDS();
      drain();
      put_flag(fo + 2 * w * 1024, tag + 1);
      if (WD == 1) lds_post(L_STG + 4 * w);
    }
  }
  if (!br) {
    IDS();
    const int od = isG ? 1 : 0, tl = 2 * w + od;
    if (!isG) { lds_wait(L_STG + 4 * w, 2); if (w) lds_wait(L_STG + 4 * w - 4, 2); else lds_wait(L_HS, 1); }
    LANES();
    const u32 lo = min(16 * g, 24);
    const u32 pk = od ? blk + O_K : w ? blk - 16384 + O_K + 16 * RS : L_SP + 1344 - 8 * RS, pvb = od ? blk + O_V : w ? blk - 16384 + O_V + 16 * RS : L_SP - 8 * RS;
    u32 qrow = blk + (16 * od + i16) * RS;
    const u32 kc = qrow + O_K, kq = pk + (i16 | 8) * RS;
    const u32 vc = blk + O_V + (16 * od + 4 * g + q4) * RS, vp = pvb + ((4 * g + q4) | 8) * RS;
    u32x4 Kp[4], Kc[4], Vm[4];
#pragma unroll
    for (int h = 0; h < 4; ++h) { Kp[h] = cat(ldsr<u32x2>(kq + lo + 40 * h), ldsr<u32x2>(kq + lo + 40 * h + 8)); Kc[h] = cat(ldsr<u32x2>(kc + lo + 40 * h), ldsr<u32x2>(kc + lo + 40 * h + 8)); }
    u32 qlo = g < 2 ? qrow + lo : L_ZERO, qhi = g < 3 ? qrow + lo + 8 : L_ZERO;
#pragma unroll
    for (int h = 0; h < 4; ++h) Vm[h] = cat(trrd(vp + 40 * h + 8 * p4), trrd(vc + 40 * h + 8 * p4));
    u32x4 Vx = cat(trrd(vp + 40 * p4 + 32), trrd(vc + 40 * p4 + 32));
    const int dl = 4 * g - i16, pm = (rt < 2 && tl == 0) ? 99 : 9;
    f32x4 Mp, Mc;
#pragma unroll
    for (int r = 0; r < 4; ++r) { Mp[r] = dl + r >= pm ? 0.f : -INFINITY; Mc[r] = (u32)(dl + r + 7) < 8u ? 0.f : -INFINITY; }
    u32 mp0 = (dl >= 9 ? 0xffffu : 0u) | (dl >= 8 ? 0xffff0000u : 0u), mp1 = (dl >= 7 ? 0xffffu : 0u) | (dl >= 6 ? 0xffff0000u : 0u);
    u32x4 ones = {0x3f803f80u, 0x3f803f80u, 0x3f803f80u, 0x3f803f80u};
    u32 so = ((10 * hg + g) >> 2) * 8192 + ((10 * hg + g) & 3) * 256 + tl * 1024 + i16 * 16, ow = qrow + 8 * g, oxa = qrow + 40 * g + 32, ora = qrow + 16 * g;
    asm volatile("" : "+v"(Kp[0]), "+v"(Kp[1]), "+v"(Kp[2]), "+v"(Kp[3]), "+v"(Kc[0]), "+v"(Kc[1]), "+v"(Kc[2]), "+v"(Kc[3]), "+v"(Vm[0]), "+v"(Vm[1]), "+v"(Vm[2]), "+v"(Vm[3]), "+v"(Vx),
                 "+v"(Mp), "+v"(Mc), "+v"(mp0), "+v"(mp1), "+v"(ones), "+v"(so), "+v"(ow), "+v"(oxa), "+v"(ora), "+v"(qlo), "+v"(qhi));
    if (!isG) lds_wait(L_STG + 4 * w, 3);
    STAMP(8);
    f32x4 sp[4], sc[4];
#define QRD(h) "ds_read_b64 v[112+4*" #h ":113+4*" #h "], %[ql] offset:40*" #h "\n\tds_read_b64 v[114+4*" #h ":115+4*" #h "], %[qh] offset:40*" #h "\n\t"
#define SMM(h, n) "s_waitcnt lgkmcnt(" #n ")\n\tv_mfma_f32_16x16x32_bf16 %[p" #h "], %[a" #h "], v[112+4*" #h ":115+4*" #h "], %[mp]\n\tv_mfma_f32_16x16x32_bf16 %[c" #h "], %[b" #h "], v[112+4*" #h ":115+4*" #h "], %[mc]\n\t"
    asm volatile(QRD(0) QRD(1) QRD(2) QRD(3) SMM(0, 6) SMM(1, 4) SMM(2, 2) SMM(3, 0) "s_nop 7"
                 : [p0] "=&v"(sp[0]), [p1] "=&v"(sp[1]), [p2] "=&v"(sp[2]), [p3] "=&v"(sp[3]), [c0] "=&v"(sc[0]), [c1] "=&v"(sc[1]), [c2] "=&v"(sc[2]), [c3] "=&v"(sc[3])
                 : [a0] "v"(Kp[0]), [a1] "v"(Kp[1]), [a2] "v"(Kp[2]), [a3] "v"(Kp[3]), [b0] "v"(Kc[0]), [b1] "v"(Kc[1]), [b2] "v"(Kc[2]), [b3] "v"(Kc[3]),
                   [ql] "v"(qlo), [qh] "v"(qhi), [mp] "v"(Mp), [mc] "v"(Mc)
                 : "memory", "v112", "v113", "v114", "v115", "v116", "v117", "v118", "v119", "v120", "v121", "v122", "v123", "v124", "v125", "v126", "v127");
    const float SC = 0.22360679774997896f * 1.4426950408889634f;
    float s[4][4], m[4];
#pragma unroll
    for (int h = 0; h < 4; ++h) {
#pragma unroll
      for (int r = 0; r < 4; ++r) s[h][r] = max2f(sp[h][r], sc[h][r]);
      m[h] = max2n(max3f(s[h][0], s[h][1], s[h][2]), s[h][3]);
    }
    {
      auto a = __builtin_amdgcn_permlane32_swap(__float_as_uint(m[0]), __float_as_uint(m[1]), false, false);
      auto c = __builtin_amdgcn_permlane32_swap(__float_as_uint(m[2]), __float_as_uint(m[3]), false, false);
      auto d = __builtin_amdgcn_permlane16_swap(__float_as_uint(max2n(__uint_as_float(a[0]), __uint_as_float(a[1]))), __float_as_uint(max2n(__uint_as_float(c[0]), __uint_as_float(c[1]))), false, false);
      const u32 f = __float_as_uint(max2f(__uint_as_float(d[0]), __uint_as_float(d[1])) * -SC);
      auto e = __builtin_amdgcn_permlane16_swap(f, f, false, false);
      auto x = __builtin_amdgcn_permlane32_swap(e[0], e[0], false, false), y = __builtin_amdgcn_permlane32_swap(e[1], e[1], false, false);
      m[0] = __uint_as_float(x[0]); m[1] = __uint_as_float(x[1]); m[2] = __uint_as_float(y[0]); m[3] = __uint_as_float(y[1]);
    }
    u32x4 P[4];
#pragma unroll
    for (int h = 0; h < 4; ++h) {
      const u32 e0 = cvtpk(__builtin_amdgcn_exp2f(fmaf(s[h][0], SC, m[h])), __builtin_amdgcn_exp2f(fmaf(s[h][1], SC, m[h])));
      const u32 e1 = cvtpk(__builtin_amdgcn_exp2f(fmaf(s[h][2], SC, m[h])), __builtin_amdgcn_exp2f(fmaf(s[h][3], SC, m[h])));
      P[h] = (u32x4){e0 & mp0, e1 & mp1, e0 & ~mp0, e1 & ~mp1};
    }
    f32x4 om[4], oe[4], sm[4];
#define PV1(h) "v_mfma_f32_16x16x32_bf16 %[s" #h "], %[one], %[p" #h "], 0\n\tv_mfma_f32_16x16x32_bf16 %[m" #h "], %[v" #h "], %[p" #h "], 0\n\t"
#define PV2(h) "v_mfma_f32_16x16x32_bf16 %[e" #h "], %[vx], %[p" #h "], 0\n\t"
    asm volatile("s_nop 1\n\t" PV1(0) PV1(1) PV1(2) PV1(3) PV2(0) PV2(1) PV2(2) PV2(3) "s_nop 7"
                 : [s0] "=&v"(sm[0]), [s1] "=&v"(sm[1]), [s2] "=&v"(sm[2]), [s3] "=&v"(sm[3]), [m0] "=&v"(om[0]), [m1] "=&v"(om[1]), [m2] "=&v"(om[2]), [m3] "=&v"(om[3]),
                   [e0] "=&v"(oe[0]), [e1] "=&v"(oe[1]), [e2] "=&v"(oe[2]), [e3] "=&v"(oe[3])
                 : [v0] "v"(Vm[0]), [v1] "v"(Vm[1]), [v2] "v"(Vm[2]), [v3] "v"(Vm[3]), [p0] "v"(P[0]), [p1] "v"(P[1]), [p2] "v"(P[2]), [p3] "v"(P[3]), [vx] "v"(Vx), [one] "v"(ones));
    float rx = 0.f;
#pragma unroll
    for (int h = 0; h < 4; ++h) {
      const float rs = __builtin_amdgcn_rcpf(sm[h][0]);
      ldsw<u32x2>(ow + 40 * h, (u32x2){cvtpk(om[h][0] * rs, om[h][1] * rs), cvtpk(om[h][2] * rs, om[h][3] * rs)});
      rx = (h == 0 || g == h) ? rs : rx;
    }
    const f32x4 ox = g < 2 ? (g == 0 ? oe[0] : oe[1]) : (g == 2 ? oe[2] : oe[3]);
    ldsw<u32x2>(oxa, (u32x2){cvtpk(ox[0] * rx, ox[1] * rx), cvtpk(ox[2] * rx, ox[3] * rx)});
    const u32x4 o0 = cat(ldsr<u32x2>(ora), ldsr<u32x2>(ora + 8)), o1 = cat(ldsr<u32x2>(ora + 64), ldsr<u32x2>(ora + 72)), o2 = cat(ldsr<u32x2>(ora + 128), ldsr<u32x2>(ora + 136));
    st16(op, so, o0); st16(op + 8192, so, o1);
    if (g < 2) st16(op + 16384, so, o2);
    lds_post(L_HS + 12);
    STAMP(9);
    drain();
    put_flag(fo + tl * 1024, tag + 1);
  }
  STAMP(10);
  if (isG) {
    IDS();
    LANES();
    const int fst = F2 && b2 ? 1024 : 128;
    const char* const fp = F2 && b2 ? ws + W_F2 + (p2 * 4 + w) * 8192 + c2g * 128 : ws + W_FO + ((b2 * 16 + p2) * 64 + 16 * w) * 128;
    u64 fv;
    asm volatile("global_load_dwordx2 %0, %1, %2 sc1" : "=&v"(fv) : "v"((lane & (15 - 8 * b2)) * fst), "s"(fp) : "memory");
    u32 oo[5], ol[5], ow = L_X + 32768 + w * 5376 + i16 * RS + 40 * g;
#pragma unroll
    for (int i = 0; i < 5; ++i) { const int G = 64 * i + lane, q = (G * 6554) >> 16, c = G - 10 * q; oo[i] = q * (2 - b2) * 2560 + c * 16; ol[i] = L_X + 32768 + w * 5376 + q * RS + c * 16; }
    u64 ob = (u64)out + (u64)((b2 ? 128 * p2 : 256 * (p2 >> 1) + (p2 & 1)) + 32 * w * (2 - b2)) * 2560 + b2 * 1280 + c2g * 160;
    asm volatile("" : "+v"(oo[0]), "+v"(oo[1]), "+v"(oo[2]), "+v"(oo[3]), "+v"(oo[4]), "+v"(ol[0]), "+v"(ol[1]), "+v"(ol[2]), "+v"(ol[3]), "+v"(ol[4]), "+v"(ow), "+s"(ob));
    {
      u32 vWb = (16 * (g >> 1) + 4 * (g & 1) + q4) * 160 + 40 * p4, vo = w * 2048 + lane * 16, vWd = vWb + 61440, vo1 = vo + 8192;
      u32 vf = (lane & (15 - 8 * b2)) * fst;
      asm volatile("" : "+v"(vWb), "+v"(vWd), "+v"(vo), "+v"(vo1), "+v"(vf));
      const u64 sXb = (u64)ip;
      u64x8 s0; u64x2 s1;
#pragma unroll
      for (int i = 0; i < 10; ++i) { const u32 p = rot2 + i; const u64 a = sXb + (p < 10 ? p : p - 10) * 16384; if (i < 8) s0[i] = a; else s1[i - 8] = a; }
      asm volatile("" : "+s"(s0), "+s"(s1));
      if (b2 == br) __builtin_amdgcn_s_sleep(FD);
      STAMP(7);
      if (AD && b2) __builtin_amdgcn_s_sleep(AD);
      if (AP && b2) {
#pragma nounroll
        for (u32 s = 0; s < SPIN_CAP; ++s) {
          if (__hip_atomic_load((GAS u64*)(fp + (c2g & APM) * 128), RLX_AGENT) == tag + 1) break;
          __builtin_amdgcn_s_sleep(AP);
        }
      }
      asm volatile("s_waitcnt vmcnt(0)" : "+v"(fv) :: "memory");
      u64 un = 0;
      if (!__all(fv == tag + 1)) {
        u32 bnd;
#define LOOK(r) "global_load_dwordx2 " r ", %[o], %[p] sc1\n\ts_sleep " PPS "\n\t"
#define SEEN(r) "s_waitcnt vmcnt(2)\n\tv_cmp_ne_u64_e32 vcc, %[t], " r "\n\ts_and_b64 %[u], vcc, %[m]\n\ts_cbranch_scc0 LFx_%=\n\t"
        asm volatile("s_mov_b32 %[b], 0x10000\n\ts_mov_b64 exec, 0xffff\n\t" LOOK("v[32:33]") LOOK("v[34:35]") "LF_%=:\n\t" LOOK("v[36:37]") SEEN("v[32:33]") LOOK("v[32:33]") SEEN("v[34:35]") LOOK("v[34:35]") SEEN("v[36:37]")
                     "s_sub_u32 %[b], %[b], 1\n\ts_cbranch_scc0 LF_%=\nLFx_%=:\n\ts_mov_b64 %[u], vcc\n\ts_mov_b64 exec, -1"
                     : [u] "=&s"(un), [b] "=&s"(bnd) : [o] "v"(vf), [p] "s"(fp), [t] "s"(tag + 1), [m] "s"((u64)TGM) : "memory", "scc", "vcc", "v32", "v33", "v34", "v35", "v36", "v37");
      }
      {
      STAMP(11);
      asm volatile(P_BODY : "=&{v[48:63]}"(c0), "=&{v[64:79]}"(c1), "=&{v[80:87]}"(c2)
                   : "{v4}"(vWb), "{v5}"(vWd), "{v6}"(vo), "{v7}"(vo1), "{v8}"(vf), "{s[44:59]}"(s0), "{s[60:63]}"(s1), "{s[64:65]}"(fp), "{s[66:67]}"(tag + 1), "{s[68:69]}"(un)
                   : "memory", "scc", "vcc", "s75", "v32", "v33", "v34", "v35", "v36", "v37", "v38", "v39", "v12","v13","v14","v15","v16","v17","v18","v19","v20","v21","v22","v23","v24","v25","v26","v27","v28","v29","v30","v31", "v96","v97","v98","v99","v100","v101","v102","v103","v104","v105","v106","v107","v108","v109","v110","v111","v112","v113","v114","v115","v116","v117","v118","v119","v120","v121","v122","v123","v124","v125","v126","v127", "a0","a1","a2","a3","a4","a5","a6","a7","a8","a9","a10","a11","a12","a13","a14","a15","a16","a17","a18","a19","a20","a21","a22","a23","a24","a25","a26","a27","a28","a29","a30","a31","a32","a33","a34","a35","a36","a37","a38","a39","a40","a41","a42","a43","a44","a45","a46","a47","a48","a49","a50","a51","a52","a53","a54","a55","a56","a57","a58","a59","a60","a61","a62","a63","a64","a65","a66","a67","a68","a69","a70","a71","a72","a73","a74","a75","a76","a77","a78","a79","a80","a81","a82","a83","a84","a85","a86","a87","a88","a89","a90","a91","a92","a93","a94","a95","a96","a97","a98","a99","a100","a101","a102","a103","a104","a105","a106","a107","a108","a109","a110","a111","a112","a113","a114","a115","a116","a117","a118","a119","a120","a121","a122","a123","a124","a125","a126","a127");
      }
    }
    STAMP(12);
    UNPACK();
#pragma unroll
    for (int n = 0; n < 10; ++n) ldsw<u32x2>(ow + (n / 5) * 16 * RS + 8 * (n % 5), (u32x2){cvtpk(v[4 * n], v[4 * n + 1]), cvtpk(v[4 * n + 2], v[4 * n + 3])});
    u32x4 r[5];
#pragma unroll
    for (int i = 0; i < 5; ++i) r[i] = cat(ldsr<u32x2>(ol[i]), ldsr<u32x2>(ol[i] + 8));
#pragma unroll
    for (int i = 0; i < 5; ++i) asm volatile("global_store_dwordx4 %0, %1, %2" OMOD :: "v"(oo[i]), "v"(r[i]), "s"(ob) : "memory");
    STAMP(13);
  } else if (b < 256) {
    LANES();
    if (WD == 2) __builtin_amdgcn_s_setprio(0);
    if (br) wait4(L_STG, WD == 1 ? 5 : 4); else wait4(L_FREE, 20);
    if (WD > 2 && br) __builtin_amdgcn_s_sleep(WD);
    if (w == 3) lds_wait(L_HS + 12, br ? 4 : 8);
    asm volatile(WO_STORE :: [a] "v"(w * 25600 + lane * 16), [b] "v"(w * 25600 + (w == 3 ? L_X - 81920 : 0) + lane * 16) : "memory");
    lds_post(L_WO);
    STAMP(12);
    if (b == 255 && w == 3) {
#pragma nounroll
      for (int k = 0; k < 4; ++k) wait_flags(F2 && k > 1 ? ws + W_F2 + ((k - 2) * 8 + (lane >> 3)) * 32768 + (lane & 7) * 1024 : ws + W_FO + (k * 8 + (lane >> 3)) * 64 * 128 + (lane & 7) * 128, 64, tag + 1, lane, 0);
      if (lane == 0) __hip_atomic_store((GAS u64*)(ws + W_EPOCH), tag + 1, RLX_AGENT);
    }
  } else {
    asm volatile(SLOW_PATHS ::: "memory");
  }
  DBG_OUT();
}

extern "C" void kernel_launch(void* const* d_in, const int* in_sizes, int n_in, void* d_out, int out_size, void* d_ws, size_t ws_size, hipStream_t stream) {
  fused_kernel<<<256, 512, 0, stream>>>(d_in[0], d_in[1], d_in[2], d_in[5], d_in[3], d_in[4], d_in[6], d_in[5], d_out, d_ws);
}
```
